```python
import torch

def _softmax(x, axis=-1):
    return torch.softmax(x, dim=axis)
def _gelu(x):
    return torch.nn.functional.gelu(x, approximate="tanh")
def _silu(x):
    return torch.nn.functional.silu(x)
def _sigmoid(x):
    return torch.sigmoid(x)
def _layernorm(x, axis=-1, eps=1e-5):
    m = x.mean(dim=axis, keepdim=True)
    v = x.var(dim=axis, keepdim=True, unbiased=False)
    return (x - m) / torch.sqrt(v + eps)
def _scatter_add(tgt, idx, src, axis=0):
    return tgt.clone().index_add_(axis, idx, src)
def _segment_sum(src, seg, n):
    out = torch.zeros((n,) + src.shape[1:], dtype=src.dtype)
    return out.index_add_(0, seg, src)
def _pad(x, pw):
    flat = tuple(v for pair in reversed(pw) for v in pair)
    return torch.nn.functional.pad(x, flat)

kernel_name = 'standalone__weight_only_int_omniquant__prompt'

def setup_inputs():

    _w = (lambda a: (lambda t: t.to(torch.bfloat16) if t.is_floating_point() else t)(torch.as_tensor(a)))
    B, S, D, F = 1, 2048, 1024, 3584
    bits = 3
    G = 64 if 64 > 0 else D
    D = (D // G) * G
    ng = D // G
    qmax = (1 << bits) - 1
    qw = torch.randint(0, qmax + 1, (D, F)).to(torch.int32)
    scales = (torch.abs((lambda *s: torch.randn(*s, dtype=torch.bfloat16) * 0.05)(ng, F)) * 0.05 + 0.01).to(float)
    zeros = torch.randint(0, qmax + 1, (ng, F)).to(float)
    act_scale = (torch.abs((lambda *s: torch.randn(*s, dtype=torch.bfloat16) * 0.05)(D)) * 0.5 + 0.5).to(float)
    return {
        "x": _w((lambda *s: torch.randn(*s, dtype=torch.bfloat16) * 0.05)(B, S, D)),
        "qw": _w(qw),
        "scales": _w(scales),
        "zeros": _w(zeros),
        "act_scale": _w(act_scale),
    }

def reference(x, qw, scales, zeros, act_scale):
    B, S, D = x.shape
    ng, F = scales.shape
    G = D // ng
    s = (lambda x, n, axis: torch.repeat_interleave(x, n, dim=axis))(scales, G, axis=0)
    z = (lambda x, n, axis: torch.repeat_interleave(x, n, dim=axis))(zeros, G, axis=0)
    w = ((lambda x, dt: x.to(dt))(qw, x.dtype) - z) * s
    xs = x * (lambda x, dt: x.to(dt))(act_scale, x.dtype)
    return torch.einsum("bsd,df->bsf", xs, w)
```

```cpp
#include <hip/hip_runtime.h>
#include <hip/hip_bf16.h>
typedef __attribute__((ext_vector_type(8))) __bf16 bf16x8;
typedef __attribute__((ext_vector_type(4))) float f32x4;
constexpr int M = 2048, K = 1024, N = 3584, G = 64;

__device__ __forceinline__ float bf2f(unsigned short h) { return __uint_as_float(((unsigned)h) << 16); }

__global__ void naive_kernel(const unsigned short* __restrict__ x, const int* __restrict__ qw,
                             const unsigned short* __restrict__ sc, const unsigned short* __restrict__ zr,
                             const unsigned short* __restrict__ as, __hip_bfloat16* __restrict__ out) {
  int wave = (blockIdx.x * blockDim.x + threadIdx.x) >> 6;
  int lane = threadIdx.x & 63;
  int tm = wave / (N / 16), tn = wave % (N / 16);
  int r = lane & 15, q = lane >> 4;
  f32x4 acc = {0, 0, 0, 0};
  int n = tn * 16 + r, m = tm * 16 + r;
  for (int k0 = 0; k0 < K; k0 += 32) {
    int g = k0 / G;
    float s = bf2f(sc[g * N + n]), z = bf2f(zr[g * N + n]);
    bf16x8 a, b;
    for (int j = 0; j < 8; ++j) {
      int k = k0 + q * 8 + j;
      float w = ((float)qw[k * N + n] - z) * s * bf2f(as[k]);
      b[j] = (__bf16)w;
      a[j] = __builtin_bit_cast(__bf16, x[m * K + k]);
    }
    acc = __builtin_amdgcn_mfma_f32_16x16x32_bf16(a, b, acc, 0, 0, 0);
  }
  for (int j = 0; j < 4; ++j) out[(tm * 16 + q * 4 + j) * N + tn * 16 + r] = __float2bfloat16(acc[j]);
}

extern "C" void kernel_launch(void* const* d_in, const int* in_sizes, int n_in, void* d_out, int out_size,
                              void* d_ws, size_t ws_size, hipStream_t stream) {
  int waves = (M / 16) * (N / 16);
  naive_kernel<<<waves / 4, 256, 0, stream>>>((const unsigned short*)d_in[0], (const int*)d_in[1],
      (const unsigned short*)d_in[2], (const unsigned short*)d_in[3], (const unsigned short*)d_in[4],
      (__hip_bfloat16*)d_out);
}
```

```cpp
#include <hip/hip_runtime.h>

constexpr int kLdsBytes = 3 * 49152;
constexpr size_t kImageBytes = 1024 * 3584 * 2;

__global__ __launch_bounds__(512, 2) void wq_gemm(const void* x, const void* qw, const void* scales, const void* zeros,
                                                  const void* act_scale, void* out, char* image, char* sync) {
  __shared__ __attribute__((aligned(1024))) char smem[kLdsBytes];
  asm volatile(
      "v_readfirstlane_b32 s44, %[tid]\n\t"
      "s_load_dwordx16 s[72:87], %[ka], 0\n\t"
      "s_getpc_b64 s[88:89]\n\t"
      ".Lpc0_%=:\n\t"
      "s_cmp_lt_u32 %[bid], 32\n\t"
      "s_cselect_b32 s90, .Lend%=-.Lpc0_%=, 0\n\t"
      "s_mov_b32 s91, 0x20000\n\t"
      "v_lshlrev_b32 v30, 7, %[tid]\n\t"
      "buffer_load_dword v31, v30, s[88:91], 0 offen\n\t"
      "s_cmp_lt_u32 %[bid], 32\n\t"
      "s_cbranch_scc1 .Lhelp%=\n\t"
      "s_cmp_ge_u32 s44, 256\n\t"
      "s_cbranch_scc1 .Lcons%=\n\t"
      "v_and_b32 v2, 63, %[tid]\n\t"
      "s_bfe_u32 s47, s44, 0x20006\n\t"
      "s_lshr_b32 s48, %[bid], 3\n\t"
      "s_sub_u32 s48, s48, 4\n\t"
      "s_mul_i32 s49, s48, 37\n\t"
      "s_lshr_b32 s49, s49, 8\n\t"
      "s_bfe_u32 s45, %[bid], 0x20001\n\t"
      "s_sub_u32 s45, s45, s49\n\t"
      "s_mul_i32 s45, s45, 7\n\t"
      "s_add_u32 s45, s45, s48\n\t"
      "s_and_b32 s48, %[bid], 1\n\t"
      "s_lshl_b32 s46, s48, 2\n\t"
      "s_add_u32 s46, s46, s49\n\t"
      "s_lshr_b32 s48, s46, 2\n\t"
      "s_bfe_u32 s49, s46, 0x10001\n\t"
      "s_mul_i32 s52, s48, 0x44444444\n\t"
      "s_mul_i32 s50, s49, 0x20020022\n\t"
      "s_xor_b32 s52, s52, s50\n\t"
      "s_xor_b32 s52, s52, 0x37526410\n\t"
      "s_mov_b32 s53, 0xfedcba98\n\t"
      "s_mov_b32 s14, 14680064\n\t"
      "s_mov_b32 s15, 0x20000\n\t"
      "s_mov_b32 s18, 114688\n\t"
      "s_mov_b32 s19, 0x20000\n\t"
      "s_mov_b32 s22, 114688\n\t"
      "s_mov_b32 s23, 0x20000\n\t"
      "s_mov_b32 s26, 2048\n\t"
      "s_mov_b32 s27, 0x20000\n\t"
      "v_and_b32 v3, 31, v2\n\t"
      "v_lshrrev_b32 v50, 5, v2\n\t"
      "v_mul_u32_u24 v32, 114688, v50\n\t"
      "v_lshl_add_u32 v32, v3, 4, v32\n\t"
      "v_lshlrev_b32 v48, 3, v3\n\t"
      "v_lshlrev_b32 v49, 4, v50\n\t"
      "v_add_u32 v33, 14336, v32\n\t"
      "v_add_u32 v34, 28672, v32\n\t"
      "v_add_u32 v35, 43008, v32\n\t"
      "v_add_u32 v36, 57344, v32\n\t"
      "v_add_u32 v37, 71680, v32\n\t"
      "v_add_u32 v38, 86016, v32\n\t"
      "v_add_u32 v39, 100352, v32\n\t"
      "s_and_b32 s56, s52, 15\n\t"
      "s_lshl_b32 s64, s56, 2\n\t"
      "s_add_u32 s64, s64, s47\n\t"
      "s_mul_i32 s59, s64, 229376\n\t"
      "s_lshl_b32 s48, s45, 9\n\t"
      "s_add_u32 s59, s59, s48\n\t"
      "s_mul_i32 s63, s56, 7168\n\t"
      "s_lshl_b32 s48, s45, 8\n\t"
      "s_add_u32 s63, s63, s48\n\t"
      "s_lshl_b32 s64, s64, 5\n\t"
      "s_waitcnt lgkmcnt(0)\n\t"
      "s_mov_b64 s[16:17], s[76:77]\n\t"
      "s_mov_b64 s[20:21], s[78:79]\n\t"
      "s_mov_b64 s[24:25], s[80:81]\n\t"
      "s_mov_b64 s[12:13], s[74:75]\n\t"
      "s_mov_b64 s[8:9], s[72:73]\n\t"
      "s_mov_b64 s[40:41], s[86:87]\n\t"
      "s_mov_b64 s[36:37], s[84:85]\n\t"
      "buffer_load_dwordx2 v[192:193], v48, s[16:19], s63 offen\n\t"
      "buffer_load_dwordx2 v[194:195], v48, s[20:23], s63 offen\n\t"
      "buffer_load_dwordx4 v[196:199], v49, s[24:27], s64 offen\n\t"
      "buffer_load_dwordx4 v[64:67], v32, s[12:15], s59 offen nt\n\t"
      "buffer_load_dwordx4 v[68:71], v33, s[12:15], s59 offen nt\n\t"
      "buffer_load_dwordx4 v[72:75], v34, s[12:15], s59 offen nt\n\t"
      "buffer_load_dwordx4 v[76:79], v35, s[12:15], s59 offen nt\n\t"
      "buffer_load_dwordx4 v[80:83], v36, s[12:15], s59 offen nt\n\t"
      "buffer_load_dwordx4 v[84:87], v37, s[12:15], s59 offen nt\n\t"
      "buffer_load_dwordx4 v[88:91], v38, s[12:15], s59 offen nt\n\t"
      "buffer_load_dwordx4 v[92:95], v39, s[12:15], s59 offen nt\n\t"
      "s_mov_b32 s10, 4194304\n\t"
      "s_mov_b32 s11, 0x20000\n\t"
      "s_mov_b32 s38, 7340032\n\t"
      "s_mov_b32 s39, 0x20000\n\t"
      "s_mov_b32 s42, 243712\n\t"
      "s_mov_b32 s43, 0x20000\n\t"
      "v_and_b32 v51, 7, v2\n\t"
      "v_lshlrev_b32 v51, 3, v51\n\t"
      "v_xor_b32 v50, v50, v2\n\t"
      "v_lshlrev_b32 v50, 4, v50\n\t"
      "s_lshl_b32 s48, s45, 2\n\t"
      "s_add_u32 s48, s48, s47\n\t"
      "s_lshl_b32 s48, s48, 7\n\t"
      "s_add_u32 s74, s48, 229376\n\t"
      "buffer_load_dwordx2 v[54:55], v51, s[40:43], s74 offen sc1\n\t"
      "v_cmp_gt_u32 vcc, 8, v2\n\t"
      "v_lshlrev_b32 v52, 3, v2\n\t"
      "s_nop 0\n\t"
      "v_cndmask_b32 v52, 2.0, v52, vcc\n\t"
      "s_lshl_b32 s48, s45, 4\n\t"
      "s_add_u32 s48, s48, s56\n\t"
      "s_lshl_b32 s48, s48, 2\n\t"
      "s_add_u32 s48, s48, s47\n\t"
      "s_lshl_b32 s78, s48, 12\n\t"
      "s_lshl_b32 s48, s45, 2\n\t"
      "s_add_u32 s48, s48, s47\n\t"
      "s_lshl_b32 s48, s48, 4\n\t"
      "s_add_u32 s48, s48, s56\n\t"
      "s_lshl_b32 s79, s48, 7\n\t"
      "s_mov_b64 s[54:55], s[52:53]\n\t"
      "s_lshl_b32 s48, s46, 2\n\t"
      "s_add_u32 s48, s48, s47\n\t"
      "s_lshl_b32 s48, s48, 17\n\t"
      "v_lshrrev_b32 v5, 3, v2\n\t"
      "v_lshl_add_u32 v5, v5, 11, s48\n\t"
      "v_lshrrev_b32 v6, 4, v2\n\t"
      "v_xor_b32 v6, v6, v2\n\t"
      "v_and_b32 v6, 7, v6\n\t"
      "v_lshl_add_u32 v5, v6, 4, v5\n\t"
      "v_xor_b32 v6, 64, v5\n\t"
      "s_lshl_b32 s67, s47, 13\n\t"
      "v_add_u32 v40, 0, v5\n\t"
      "v_add_u32 v41, 15360, v6\n\t"
      "v_add_u32 v42, 30720, v5\n\t"
      "v_add_u32 v43, 46080, v6\n\t"
      "v_add_u32 v44, 65536, v5\n\t"
      "v_add_u32 v45, 80896, v6\n\t"
      "v_add_u32 v46, 96256, v5\n\t"
      "v_add_u32 v47, 111616, v6\n\t"
      "v_and_b32 v7, 15, v2\n\t"
      "v_lshrrev_b32 v8, 4, v2\n\t"
      "v_cmp_gt_u32 vcc, 10, v7\n\t"
      "s_mul_i32 s48, s45, 10\n\t"
      "v_add_u32 v7, s48, v7\n\t"
      "v_cmp_gt_u32 s[84:85], 3, v8\n\t"
      "s_mul_i32 s48, s47, 3\n\t"
      "s_and_b64 vcc, vcc, s[84:85]\n\t"
      "s_add_u32 s48, s48, 4\n\t"
      "v_add_u32 v8, s48, v8\n\t"
      "s_movk_i32 s49, 0x100\n\t"
      "v_cmp_gt_u32 s[84:85], s49, v7\n\t"
      "s_nop 0\n\t"
      "s_and_b64 vcc, vcc, s[84:85]\n\t"
      "v_cmp_gt_u32 s[84:85], 16, v8\n\t"
      "v_lshlrev_b32 v8, 2, v8\n\t"
      "s_and_b64 vcc, vcc, s[84:85]\n\t"
      "v_lshrrev_b64 v[10:11], v8, s[52:53]\n\t"
      "v_and_b32 v8, 15, v10\n\t"
      "s_lshl_b32 s49, s46, 19\n\t"
      "v_lshl_add_u32 v7, v7, 11, s49\n\t"
      "v_lshl_add_u32 v7, v8, 7, v7\n\t"
      "v_cndmask_b32 v61, 2.0, v7, vcc\n\t"
      "s_mov_b32 s70, 0\n\t"
      "s_mov_b32 s71, 0\n\t"
      "s_lshl_b32 s72, s45, 18\n\t"
      "s_lshl_b32 s48, s47, 12\n\t"
      "s_add_u32 s72, s72, s48\n\t"
      "s_sub_u32 s76, 32768, s48\n\t"
      "s_add_u32 s48, s48, 32768\n\t"
      "v_add_u32 v53, s48, v50\n\t"
      "s_lshl_b32 s73, s45, 2\n\t"
      "s_add_u32 s73, s73, s47\n\t"
      "s_lshl_b32 s73, s73, 8\n\t"
      "s_add_u32 s73, s73, s46\n\t"
      "s_lshl_b32 s73, s73, 3\n\t"
      "v_lshlrev_b32 v58, 4, v2\n\t"
      "v_lshlrev_b32 v59, 7, v2\n\t"
      "v_cmp_eq_u32 vcc, s46, v2\n\t"
      "v_lshlrev_b32 v60, 3, v2\n\t"
      "s_nop 0\n\t"
      "v_cndmask_b32 v60, 2.0, v60, vcc\n\t"
      "s_lshr_b32 s48, s47, 1\n\t"
      "s_and_b32 s49, s46, 1\n\t"
      "s_cmp_eq_u32 s48, s49\n\t"
      "s_cselect_b32 s48, 0, 0x40000000\n\t"
      "v_or_b32 v50, s48, v50\n\t"
      "v_or_b32 v52, s48, v52\n\t"
      "s_waitcnt vmcnt(9)\n\t"
      "v_lshlrev_b32 v224, 16, v192\n\t"
      "v_and_b32 v225, 0xffff0000, v192\n\t"
      "v_lshlrev_b32 v228, 16, v194\n\t"
      "v_and_b32 v229, 0xffff0000, v194\n\t"
      "v_lshlrev_b32 v226, 16, v193\n\t"
      "v_and_b32 v227, 0xffff0000, v193\n\t"
      "v_lshlrev_b32 v230, 16, v195\n\t"
      "v_and_b32 v231, 0xffff0000, v195\n\t"
      "v_lshlrev_b32 v232, 16, v196\n\t"
      "v_and_b32 v233, 0xffff0000, v196\n\t"
      "v_lshlrev_b32 v234, 16, v197\n\t"
      "v_and_b32 v235, 0xffff0000, v197\n\t"
      "v_lshlrev_b32 v236, 16, v198\n\t"
      "v_and_b32 v237, 0xffff0000, v198\n\t"
      "v_lshlrev_b32 v238, 16, v199\n\t"
      "v_and_b32 v239, 0xffff0000, v199\n\t"
      "v_mul_f32 v228, -v228, v224\n\t"
      "v_mul_f32 v229, -v229, v225\n\t"
      "v_mul_f32 v230, -v230, v226\n\t"
      "v_mul_f32 v231, -v231, v227\n\t"
      "s_waitcnt vmcnt(7)\n\t"
      "v_cvt_f32_i32 v64, v64\n\t"
      "v_cvt_f32_i32 v68, v68\n\t"
      "v_cvt_f32_i32 v65, v65\n\t"
      "v_cvt_f32_i32 v69, v69\n\t"
      "v_cvt_f32_i32 v66, v66\n\t"
      "v_cvt_f32_i32 v70, v70\n\t"
      "v_cvt_f32_i32 v67, v67\n\t"
      "v_cvt_f32_i32 v71, v71\n\t"
      "v_fma_f32 v64, v64, v224, v228\n\t"
      "v_fma_f32 v68, v68, v224, v228\n\t"
      "v_fma_f32 v65, v65, v225, v229\n\t"
      "v_fma_f32 v69, v69, v225, v229\n\t"
      "v_fma_f32 v66, v66, v226, v230\n\t"
      "v_fma_f32 v70, v70, v226, v230\n\t"
      "v_fma_f32 v67, v67, v227, v231\n\t"
      "v_fma_f32 v71, v71, v227, v231\n\t"
      "v_mul_f32 v64, v64, v232\n\t"
      "v_mul_f32 v68, v68, v233\n\t"
      "v_mul_f32 v65, v65, v232\n\t"
      "v_mul_f32 v69, v69, v233\n\t"
      "v_mul_f32 v66, v66, v232\n\t"
      "v_mul_f32 v70, v70, v233\n\t"
      "v_mul_f32 v67, v67, v232\n\t"
      "v_mul_f32 v71, v71, v233\n\t"
      "v_cvt_pk_bf16_f32 v240, v64, v68\n\t"
      "v_cvt_pk_bf16_f32 v244, v65, v69\n\t"
      "v_cvt_pk_bf16_f32 v248, v66, v70\n\t"
      "v_cvt_pk_bf16_f32 v252, v67, v71\n\t"
      "s_waitcnt vmcnt(5)\n\t"
      "v_cvt_f32_i32 v72, v72\n\t"
      "v_cvt_f32_i32 v76, v76\n\t"
      "v_cvt_f32_i32 v73, v73\n\t"
      "v_cvt_f32_i32 v77, v77\n\t"
      "v_cvt_f32_i32 v74, v74\n\t"
      "v_cvt_f32_i32 v78, v78\n\t"
      "v_cvt_f32_i32 v75, v75\n\t"
      "v_cvt_f32_i32 v79, v79\n\t"
      "v_fma_f32 v72, v72, v224, v228\n\t"
      "v_fma_f32 v76, v76, v224, v228\n\t"
      "v_fma_f32 v73, v73, v225, v229\n\t"
      "v_fma_f32 v77, v77, v225, v229\n\t"
      "v_fma_f32 v74, v74, v226, v230\n\t"
      "v_fma_f32 v78, v78, v226, v230\n\t"
      "v_fma_f32 v75, v75, v227, v231\n\t"
      "v_fma_f32 v79, v79, v227, v231\n\t"
      "v_mul_f32 v72, v72, v234\n\t"
      "v_mul_f32 v76, v76, v235\n\t"
      "v_mul_f32 v73, v73, v234\n\t"
      "v_mul_f32 v77, v77, v235\n\t"
      "v_mul_f32 v74, v74, v234\n\t"
      "v_mul_f32 v78, v78, v235\n\t"
      "v_mul_f32 v75, v75, v234\n\t"
      "v_mul_f32 v79, v79, v235\n\t"
      "v_cvt_pk_bf16_f32 v241, v72, v76\n\t"
      "v_cvt_pk_bf16_f32 v245, v73, v77\n\t"
      "v_cvt_pk_bf16_f32 v249, v74, v78\n\t"
      "v_cvt_pk_bf16_f32 v253, v75, v79\n\t"
      "s_waitcnt vmcnt(3)\n\t"
      "v_cvt_f32_i32 v80, v80\n\t"
      "v_cvt_f32_i32 v84, v84\n\t"
      "v_cvt_f32_i32 v81, v81\n\t"
      "v_cvt_f32_i32 v85, v85\n\t"
      "v_cvt_f32_i32 v82, v82\n\t"
      "v_cvt_f32_i32 v86, v86\n\t"
      "v_cvt_f32_i32 v83, v83\n\t"
      "v_cvt_f32_i32 v87, v87\n\t"
      "v_fma_f32 v80, v80, v224, v228\n\t"
      "v_fma_f32 v84, v84, v224, v228\n\t"
      "v_fma_f32 v81, v81, v225, v229\n\t"
      "v_fma_f32 v85, v85, v225, v229\n\t"
      "v_fma_f32 v82, v82, v226, v230\n\t"
      "v_fma_f32 v86, v86, v226, v230\n\t"
      "v_fma_f32 v83, v83, v227, v231\n\t"
      "v_fma_f32 v87, v87, v227, v231\n\t"
      "v_mul_f32 v80, v80, v236\n\t"
      "v_mul_f32 v84, v84, v237\n\t"
      "v_mul_f32 v81, v81, v236\n\t"
      "v_mul_f32 v85, v85, v237\n\t"
      "v_mul_f32 v82, v82, v236\n\t"
      "v_mul_f32 v86, v86, v237\n\t"
      "v_mul_f32 v83, v83, v236\n\t"
      "v_mul_f32 v87, v87, v237\n\t"
      "v_cvt_pk_bf16_f32 v242, v80, v84\n\t"
      "v_cvt_pk_bf16_f32 v246, v81, v85\n\t"
      "v_cvt_pk_bf16_f32 v250, v82, v86\n\t"
      "v_cvt_pk_bf16_f32 v254, v83, v87\n\t"
      "s_waitcnt vmcnt(1)\n\t"
      "v_cvt_f32_i32 v88, v88\n\t"
      "v_cvt_f32_i32 v92, v92\n\t"
      "v_cvt_f32_i32 v89, v89\n\t"
      "v_cvt_f32_i32 v93, v93\n\t"
      "v_cvt_f32_i32 v90, v90\n\t"
      "v_cvt_f32_i32 v94, v94\n\t"
      "v_cvt_f32_i32 v91, v91\n\t"
      "v_cvt_f32_i32 v95, v95\n\t"
      "v_fma_f32 v88, v88, v224, v228\n\t"
      "v_fma_f32 v92, v92, v224, v228\n\t"
      "v_fma_f32 v89, v89, v225, v229\n\t"
      "v_fma_f32 v93, v93, v225, v229\n\t"
      "v_fma_f32 v90, v90, v226, v230\n\t"
      "v_fma_f32 v94, v94, v226, v230\n\t"
      "v_fma_f32 v91, v91, v227, v231\n\t"
      "v_fma_f32 v95, v95, v227, v231\n\t"
      "v_mul_f32 v88, v88, v238\n\t"
      "v_mul_f32 v92, v92, v239\n\t"
      "v_mul_f32 v89, v89, v238\n\t"
      "v_mul_f32 v93, v93, v239\n\t"
      "v_mul_f32 v90, v90, v238\n\t"
      "v_mul_f32 v94, v94, v239\n\t"
      "v_mul_f32 v91, v91, v238\n\t"
      "v_mul_f32 v95, v95, v239\n\t"
      "v_cvt_pk_bf16_f32 v243, v88, v92\n\t"
      "buffer_store_dwordx4 v[240:243], v50, s[36:39], s78 offen offset:0 sc1\n\t"
      "ds_write_b128 v53, v[240:243] offset:0\n\t"
      "v_cvt_pk_bf16_f32 v247, v89, v93\n\t"
      "buffer_store_dwordx4 v[244:247], v50, s[36:39], s78 offen offset:1024 sc1\n\t"
      "ds_write_b128 v53, v[244:247] offset:1024\n\t"
      "v_cvt_pk_bf16_f32 v251, v90, v94\n\t"
      "buffer_store_dwordx4 v[248:251], v50, s[36:39], s78 offen offset:2048 sc1\n\t"
      "ds_write_b128 v53, v[248:251] offset:2048\n\t"
      "v_cvt_pk_bf16_f32 v255, v91, v95\n\t"
      "buffer_store_dwordx4 v[252:255], v50, s[36:39], s78 offen offset:3072 sc1\n\t"
      "ds_write_b128 v53, v[252:255] offset:3072\n\t"
      "s_bfe_u32 s66, s52, 0x40000\n\t"
      "s_add_u32 m0, s67, 0\n\t"
      "s_lshl_b32 s66, s66, 7\n\t"
      "buffer_load_dwordx4 v40, s[8:11], s66 offen offset:0 lds\n\t"
      "buffer_load_dwordx4 v41, s[8:11], s66 offen offset:1024 lds\n\t"
      "buffer_load_dwordx4 v42, s[8:11], s66 offen offset:2048 lds\n\t"
      "buffer_load_dwordx4 v43, s[8:11], s66 offen offset:3072 lds\n\t"
      "s_bfe_u32 s66, s52, 0x40000\n\t"
      "s_add_u32 m0, s67, 4096\n\t"
      "s_lshl_b32 s66, s66, 7\n\t"
      "buffer_load_dwordx4 v44, s[8:11], s66 offen offset:0 lds\n\t"
      "buffer_load_dwordx4 v45, s[8:11], s66 offen offset:1024 lds\n\t"
      "buffer_load_dwordx4 v46, s[8:11], s66 offen offset:2048 lds\n\t"
      "buffer_load_dwordx4 v47, s[8:11], s66 offen offset:3072 lds\n\t"
      "s_waitcnt vmcnt(8)\n\t"
      "v_add_u32 v54, 1, v54\n\t"
      "buffer_store_dwordx2 v[54:55], v52, s[40:43], s79 offen sc1\n\t"
      "v_readlane_b32 s68, v54, s46\n\t"
      "v_readlane_b32 s69, v55, s46\n\t"
      "s_bfe_u32 s66, s52, 0x40004\n\t"
      "s_add_u32 m0, s67, 49152\n\t"
      "s_lshl_b32 s66, s66, 7\n\t"
      "buffer_load_dwordx4 v40, s[8:11], s66 offen offset:0 lds\n\t"
      "buffer_load_dwordx4 v41, s[8:11], s66 offen offset:1024 lds\n\t"
      "buffer_load_dwordx4 v42, s[8:11], s66 offen offset:2048 lds\n\t"
      "buffer_load_dwordx4 v43, s[8:11], s66 offen offset:3072 lds\n\t"
      "s_bfe_u32 s66, s52, 0x40004\n\t"
      "s_add_u32 m0, s67, 53248\n\t"
      "s_lshl_b32 s66, s66, 7\n\t"
      "buffer_load_dwordx4 v44, s[8:11], s66 offen offset:0 lds\n\t"
      "buffer_load_dwordx4 v45, s[8:11], s66 offen offset:1024 lds\n\t"
      "buffer_load_dwordx4 v46, s[8:11], s66 offen offset:2048 lds\n\t"
      "buffer_load_dwordx4 v47, s[8:11], s66 offen offset:3072 lds\n\t"
      "s_waitcnt vmcnt(9) lgkmcnt(0)\n\t"
      "s_barrier\n\t"
      "s_bfe_u32 s66, s52, 0x40008\n\t"
      "s_add_u32 m0, s67, 98304\n\t"
      "s_lshl_b32 s66, s66, 7\n\t"
      "buffer_load_dwordx4 v40, s[8:11], s66 offen offset:0 lds\n\t"
      "buffer_load_dwordx4 v41, s[8:11], s66 offen offset:1024 lds\n\t"
      "buffer_load_dwordx4 v42, s[8:11], s66 offen offset:2048 lds\n\t"
      "buffer_load_dwordx4 v43, s[8:11], s66 offen offset:3072 lds\n\t"
      "s_bfe_u32 s66, s52, 0x40008\n\t"
      "s_add_u32 m0, s67, 102400\n\t"
      "s_lshl_b32 s66, s66, 7\n\t"
      "buffer_load_dwordx4 v44, s[8:11], s66 offen offset:0 lds\n\t"
      "buffer_load_dwordx4 v45, s[8:11], s66 offen offset:1024 lds\n\t"
      "buffer_load_dwordx4 v46, s[8:11], s66 offen offset:2048 lds\n\t"
      "buffer_load_dwordx4 v47, s[8:11], s66 offen offset:3072 lds\n\t"
      "s_waitcnt vmcnt(8)\n\t"
      "s_barrier\n\t"
      "s_mov_b32 s77, 0\n\t"
      "s_bfe_u32 s48, s52, 0x40008\n\t"
      "s_bitset1_b32 s77, s48\n\t"
      "s_bfe_u32 s48, s52, 0x4000c\n\t"
      "s_bitset1_b32 s77, s48\n\t"
      "s_bfe_u32 s48, s52, 0x40010\n\t"
      "s_bitset1_b32 s77, s48\n\t"
      "s_mov_b32 vcc_lo, s77\n\t"
      "s_mov_b32 vcc_hi, 0\n\t"
      "v_cndmask_b32 v14, 2.0, v59, vcc\n\t"
      "buffer_load_dwordx2 v[62:63], v14, s[40:43], s73 offen sc1\n\t"
      "s_bfe_u32 s66, s52, 0x4000c\n\t"
      "s_add_u32 m0, s67, 0\n\t"
      "s_lshl_b32 s66, s66, 7\n\t"
      "buffer_load_dwordx4 v40, s[8:11], s66 offen offset:0 lds\n\t"
      "buffer_load_dwordx4 v41, s[8:11], s66 offen offset:1024 lds\n\t"
      "buffer_load_dwordx4 v42, s[8:11], s66 offen offset:2048 lds\n\t"
      "buffer_load_dwordx4 v43, s[8:11], s66 offen offset:3072 lds\n\t"
      "s_bfe_u32 s66, s52, 0x4000c\n\t"
      "s_add_u32 m0, s67, 4096\n\t"
      "s_lshl_b32 s66, s66, 7\n\t"
      "buffer_load_dwordx4 v44, s[8:11], s66 offen offset:0 lds\n\t"
      "buffer_load_dwordx4 v45, s[8:11], s66 offen offset:1024 lds\n\t"
      "buffer_load_dwordx4 v46, s[8:11], s66 offen offset:2048 lds\n\t"
      "buffer_load_dwordx4 v47, s[8:11], s66 offen offset:3072 lds\n\t"
      "s_waitcnt vmcnt(8)\n\t"
      "v_cmp_eq_u64 vcc, s[68:69], v[62:63]\n\t"
      "s_nop 4\n\t"
      "s_and_b32 s48, vcc_lo, s77\n\t"
      "s_or_b32 s70, s70, s48\n\t"
      "s_bfe_u32 s87, s52, 0x40008\n\t"
      "s_add_u32 s51, s67, 98304\n\t"
      ".Lq1_%=:\n\t"
      "s_bitcmp1_b32 s70, s87\n\t"
      "s_cbranch_scc1 .Lr1_%=\n\t"
      "s_lshl_b32 s77, 1, s87\n\t"
      "s_mov_b32 vcc_lo, s77\n\t"
      "s_mov_b32 vcc_hi, 0\n\t"
      "v_cndmask_b32 v14, 2.0, v59, vcc\n\t"
      "buffer_load_dwordx2 v[62:63], v14, s[40:43], s73 offen sc1\n\t"
      "s_add_u32 s71, s71, 1\n\t"
      "s_waitcnt vmcnt(0)\n\t"
      "v_cmp_eq_u64 vcc, s[68:69], v[62:63]\n\t"
      "s_nop 4\n\t"
      "s_and_b32 s48, vcc_lo, s77\n\t"
      "s_or_b32 s70, s70, s48\n\t"
      "s_cmp_lt_u32 s71, 0x100000\n\t"
      "s_cbranch_scc1 .Lq1_%=\n\t"
      ".Lr1_%=:\n\t"
      "s_lshl_b32 s48, s87, 14\n\t"
      "s_add_u32 m0, s51, s76\n\t"
      "s_add_u32 s48, s48, s72\n\t"
      "buffer_load_dwordx4 v58, s[36:39], s48 offen offset:0 sc1 lds\n\t"
      "buffer_load_dwordx4 v58, s[36:39], s48 offen offset:1024 sc1 lds\n\t"
      "buffer_load_dwordx4 v58, s[36:39], s48 offen offset:2048 sc1 lds\n\t"
      "buffer_load_dwordx4 v58, s[36:39], s48 offen offset:3072 sc1 lds\n\t"
      "s_bfe_u32 s87, s52, 0x4000c\n\t"
      "s_add_u32 s51, s67, 0\n\t"
      ".Lq2_%=:\n\t"
      "s_bitcmp1_b32 s70, s87\n\t"
      "s_cbranch_scc1 .Lr2_%=\n\t"
      "s_lshl_b32 s77, 1, s87\n\t"
      "s_mov_b32 vcc_lo, s77\n\t"
      "s_mov_b32 vcc_hi, 0\n\t"
      "v_cndmask_b32 v14, 2.0, v59, vcc\n\t"
      "buffer_load_dwordx2 v[62:63], v14, s[40:43], s73 offen sc1\n\t"
      "s_add_u32 s71, s71, 1\n\t"
      "s_waitcnt vmcnt(0)\n\t"
      "v_cmp_eq_u64 vcc, s[68:69], v[62:63]\n\t"
      "s_nop 4\n\t"
      "s_and_b32 s48, vcc_lo, s77\n\t"
      "s_or_b32 s70, s70, s48\n\t"
      "s_cmp_lt_u32 s71, 0x100000\n\t"
      "s_cbranch_scc1 .Lq2_%=\n\t"
      ".Lr2_%=:\n\t"
      "s_lshl_b32 s48, s87, 14\n\t"
      "s_add_u32 m0, s51, s76\n\t"
      "s_add_u32 s48, s48, s72\n\t"
      "buffer_load_dwordx4 v58, s[36:39], s48 offen offset:0 sc1 lds\n\t"
      "buffer_load_dwordx4 v58, s[36:39], s48 offen offset:1024 sc1 lds\n\t"
      "buffer_load_dwordx4 v58, s[36:39], s48 offen offset:2048 sc1 lds\n\t"
      "buffer_load_dwordx4 v58, s[36:39], s48 offen offset:3072 sc1 lds\n\t"
      "s_lshr_b64 s[54:55], s[54:55], 16\n\t"
      "s_mov_b32 s77, 0\n\t"
      "s_bfe_u32 s48, s54, 0x40000\n\t"
      "s_bitset1_b32 s77, s48\n\t"
      "s_andn2_b32 s77, s77, s70\n\t"
      "s_mov_b32 vcc_lo, s77\n\t"
      "s_mov_b32 vcc_hi, 0\n\t"
      "v_cndmask_b32 v14, 2.0, v59, vcc\n\t"
      "buffer_load_dwordx2 v[62:63], v14, s[40:43], s73 offen sc1\n\t"
      "buffer_load_dword v13, v61, s[8:11], 0 offen\n\t"
      "s_waitcnt vmcnt(6)\n\t"
      "s_barrier\n\t"
      "s_add_u32 s67, s67, 49152\n\t"
      "s_mov_b32 s75, 4\n\t"
      ".Ll_loop%=:\n\t"
      "s_and_b32 s65, s54, 15\n\t"
      "s_mov_b32 m0, s67\n\t"
      "s_lshl_b32 s66, s65, 7\n\t"
      "buffer_load_dwordx4 v40, s[8:11], s66 offen offset:0 lds\n\t"
      "buffer_load_dwordx4 v41, s[8:11], s66 offen offset:1024 lds\n\t"
      "s_lshr_b64 s[54:55], s[54:55], 4\n\t"
      "buffer_load_dwordx4 v42, s[8:11], s66 offen offset:2048 lds\n\t"
      "buffer_load_dwordx4 v43, s[8:11], s66 offen offset:3072 lds\n\t"
      "s_add_u32 m0, s67, 0x1000\n\t"
      "s_nop 0\n\t"
      "buffer_load_dwordx4 v44, s[8:11], s66 offen offset:0 lds\n\t"
      "buffer_load_dwordx4 v45, s[8:11], s66 offen offset:1024 lds\n\t"
      "buffer_load_dwordx4 v46, s[8:11], s66 offen offset:2048 lds\n\t"
      "buffer_load_dwordx4 v47, s[8:11], s66 offen offset:3072 lds\n\t"
      "s_waitcnt vmcnt(8)\n\t"
      "v_cmp_eq_u64 vcc, s[68:69], v[62:63]\n\t"
      "s_nop 4\n\t"
      "s_and_b32 s48, vcc_lo, s77\n\t"
      "s_or_b32 s70, s70, s48\n\t"
      "s_bitcmp1_b32 s70, s65\n\t"
      "s_cbranch_scc0 .Ll_late%=\n\t"
      "s_lshl_b32 s48, s65, 14\n\t"
      "s_add_u32 m0, s67, s76\n\t"
      "s_add_u32 s48, s48, s72\n\t"
      "buffer_load_dwordx4 v58, s[36:39], s48 offen offset:0 sc1 lds\n\t"
      "buffer_load_dwordx4 v58, s[36:39], s48 offen offset:1024 sc1 lds\n\t"
      "buffer_load_dwordx4 v58, s[36:39], s48 offen offset:2048 sc1 lds\n\t"
      "buffer_load_dwordx4 v58, s[36:39], s48 offen offset:3072 sc1 lds\n\t"
      "s_mov_b32 s77, 0\n\t"
      "s_bfe_u32 s48, s54, 0x40000\n\t"
      "s_bitset1_b32 s77, s48\n\t"
      "s_cmp_eq_u32 s75, 5\n\t"
      "s_cselect_b32 s48, 0xff, 0\n\t"
      "s_or_b32 s77, s77, s48\n\t"
      "s_andn2_b32 s77, s77, s70\n\t"
      "s_mov_b32 vcc_lo, s77\n\t"
      "s_mov_b32 vcc_hi, 0\n\t"
      "v_cndmask_b32 v14, 2.0, v59, vcc\n\t"
      "buffer_load_dwordx2 v[62:63], v14, s[40:43], s73 offen sc1\n\t"
      "s_barrier\n\t"
      ".Ll_next%=:\n\t"
      "s_add_u32 s67, s67, 49152\n\t"
      "s_cmp_ge_u32 s67, 147456\n\t"
      "s_cselect_b32 s48, 147456, 0\n\t"
      "s_sub_u32 s67, s67, s48\n\t"
      "s_add_u32 s75, s75, 1\n\t"
      "s_cmp_lt_u32 s75, 16\n\t"
      "s_cbranch_scc1 .Ll_loop%=\n\t"
      "s_branch .Ll_end%=\n\t"
      ".Ll_late%=:\n\t"
      "s_lshl_b32 s77, 1, s65\n\t"
      "s_mov_b32 vcc_lo, s77\n\t"
      "s_mov_b32 vcc_hi, 0\n\t"
      "v_cndmask_b32 v14, 2.0, v59, vcc\n\t"
      "buffer_load_dwordx2 v[62:63], v14, s[40:43], s73 offen sc1\n\t"
      "s_barrier\n\t"
      "s_waitcnt vmcnt(0)\n\t"
      "v_cmp_eq_u64 vcc, s[68:69], v[62:63]\n\t"
      "s_nop 4\n\t"
      "s_and_b32 s48, vcc_lo, s77\n\t"
      "s_or_b32 s70, s70, s48\n\t"
      ".Lq3_%=:\n\t"
      "s_bitcmp1_b32 s70, s65\n\t"
      "s_cbranch_scc1 .Lr3_%=\n\t"
      "s_lshl_b32 s77, 1, s65\n\t"
      "s_mov_b32 vcc_lo, s77\n\t"
      "s_mov_b32 vcc_hi, 0\n\t"
      "v_cndmask_b32 v14, 2.0, v59, vcc\n\t"
      "buffer_load_dwordx2 v[62:63], v14, s[40:43], s73 offen sc1\n\t"
      "s_add_u32 s71, s71, 1\n\t"
      "s_waitcnt vmcnt(0)\n\t"
      "v_cmp_eq_u64 vcc, s[68:69], v[62:63]\n\t"
      "s_nop 4\n\t"
      "s_and_b32 s48, vcc_lo, s77\n\t"
      "s_or_b32 s70, s70, s48\n\t"
      "s_cmp_lt_u32 s71, 0x100000\n\t"
      "s_cbranch_scc1 .Lq3_%=\n\t"
      ".Lr3_%=:\n\t"
      "s_lshl_b32 s48, s65, 14\n\t"
      "s_add_u32 m0, s67, s76\n\t"
      "s_add_u32 s48, s48, s72\n\t"
      "buffer_load_dwordx4 v58, s[36:39], s48 offen offset:0 sc1 lds\n\t"
      "buffer_load_dwordx4 v58, s[36:39], s48 offen offset:1024 sc1 lds\n\t"
      "buffer_load_dwordx4 v58, s[36:39], s48 offen offset:2048 sc1 lds\n\t"
      "buffer_load_dwordx4 v58, s[36:39], s48 offen offset:3072 sc1 lds\n\t"
      "s_mov_b32 s77, 0\n\t"
      "s_bfe_u32 s48, s54, 0x40000\n\t"
      "s_bitset1_b32 s77, s48\n\t"
      "s_cmp_eq_u32 s75, 5\n\t"
      "s_cselect_b32 s48, 0xff, 0\n\t"
      "s_or_b32 s77, s77, s48\n\t"
      "s_andn2_b32 s77, s77, s70\n\t"
      "s_mov_b32 vcc_lo, s77\n\t"
      "s_mov_b32 vcc_hi, 0\n\t"
      "v_cndmask_b32 v14, 2.0, v59, vcc\n\t"
      "buffer_load_dwordx2 v[62:63], v14, s[40:43], s73 offen sc1\n\t"
      "s_branch .Ll_next%=\n\t"
      ".Ll_end%=:\n\t"
      "s_waitcnt vmcnt(0)\n\t"
      "s_barrier\n\t"
      "v_cmp_eq_u64 vcc, s[68:69], v[62:63]\n\t"
      "s_nop 4\n\t"
      "s_and_b32 s48, vcc_lo, s77\n\t"
      "s_or_b32 s70, s70, s48\n\t"
      ".Lq4_%=:\n\t"
      "s_andn2_b32 s77, 0xffff, s70\n\t"
      "s_cbranch_scc0 .Lr4_%=\n\t"
      "s_mov_b32 vcc_lo, s77\n\t"
      "s_mov_b32 vcc_hi, 0\n\t"
      "v_cndmask_b32 v14, 2.0, v59, vcc\n\t"
      "buffer_load_dwordx2 v[62:63], v14, s[40:43], s73 offen sc1\n\t"
      "s_add_u32 s71, s71, 1\n\t"
      "s_waitcnt vmcnt(0)\n\t"
      "v_cmp_eq_u64 vcc, s[68:69], v[62:63]\n\t"
      "s_nop 4\n\t"
      "s_and_b32 s48, vcc_lo, s77\n\t"
      "s_or_b32 s70, s70, s48\n\t"
      "s_cmp_lt_u32 s71, 0x100000\n\t"
      "s_cbranch_scc1 .Lq4_%=\n\t"
      ".Lr4_%=:\n\t"
      "buffer_store_dwordx2 v[54:55], v60, s[40:43], s74 offen sc1\n\t"
      "s_endpgm\n\t"
      ".p2align 12\n\t"
      ".fill (4096 + 0 - (.Lc_loop%=-.Lcons%=)) / 4, 4, 3212836864\n\t"
      ".Lcons%=:\n\t"
      "v_and_b32 v2, 63, %[tid]\n\t"
      "s_bfe_u32 s47, s44, 0x20006\n\t"
      "s_lshr_b32 s48, %[bid], 3\n\t"
      "s_sub_u32 s48, s48, 4\n\t"
      "s_mul_i32 s49, s48, 37\n\t"
      "s_lshr_b32 s49, s49, 8\n\t"
      "s_bfe_u32 s45, %[bid], 0x20001\n\t"
      "s_sub_u32 s45, s45, s49\n\t"
      "s_mul_i32 s45, s45, 7\n\t"
      "s_add_u32 s45, s45, s48\n\t"
      "s_and_b32 s48, %[bid], 1\n\t"
      "s_lshl_b32 s46, s48, 2\n\t"
      "s_add_u32 s46, s46, s49\n\t"
      "s_lshr_b32 s48, s46, 2\n\t"
      "s_bfe_u32 s49, s46, 0x10001\n\t"
      "s_mul_i32 s52, s48, 0x44444444\n\t"
      "s_mul_i32 s50, s49, 0x20020022\n\t"
      "s_xor_b32 s52, s52, s50\n\t"
      "s_xor_b32 s52, s52, 0x37526410\n\t"
      "s_mov_b32 s53, 0xfedcba98\n\t"
      "s_mov_b32 s14, 14680064\n\t"
      "s_mov_b32 s15, 0x20000\n\t"
      "s_mov_b32 s18, 114688\n\t"
      "s_mov_b32 s19, 0x20000\n\t"
      "s_mov_b32 s22, 114688\n\t"
      "s_mov_b32 s23, 0x20000\n\t"
      "s_mov_b32 s26, 2048\n\t"
      "s_mov_b32 s27, 0x20000\n\t"
      "s_mov_b32 s42, 243712\n\t"
      "s_mov_b32 s43, 0x20000\n\t"
      "v_and_b32 v3, 31, v2\n\t"
      "v_lshrrev_b32 v10, 5, v2\n\t"
      "v_mul_u32_u24 v104, 114688, v10\n\t"
      "v_lshl_add_u32 v104, v3, 4, v104\n\t"
      "v_lshlrev_b32 v112, 3, v3\n\t"
      "v_lshlrev_b32 v113, 4, v10\n\t"
      "v_add_u32 v105, 14336, v104\n\t"
      "v_add_u32 v106, 28672, v104\n\t"
      "v_add_u32 v107, 43008, v104\n\t"
      "v_add_u32 v108, 57344, v104\n\t"
      "v_add_u32 v109, 71680, v104\n\t"
      "v_add_u32 v110, 86016, v104\n\t"
      "v_add_u32 v111, 100352, v104\n\t"
      "v_and_b32 v115, 7, v2\n\t"
      "v_lshlrev_b32 v115, 3, v115\n\t"
      "v_xor_b32 v10, v10, v2\n\t"
      "v_lshlrev_b32 v10, 4, v10\n\t"
      "s_bfe_u32 s56, s52, 0x40004\n\t"
      "s_lshl_b32 s64, s56, 2\n\t"
      "s_add_u32 s64, s64, s47\n\t"
      "s_mul_i32 s59, s64, 229376\n\t"
      "s_lshl_b32 s48, s45, 9\n\t"
      "s_add_u32 s59, s59, s48\n\t"
      "s_mul_i32 s63, s56, 7168\n\t"
      "s_lshl_b32 s48, s45, 8\n\t"
      "s_add_u32 s63, s63, s48\n\t"
      "s_lshl_b32 s64, s64, 5\n\t"
      "s_waitcnt lgkmcnt(0)\n\t"
      "s_mov_b64 s[40:41], s[86:87]\n\t"
      "s_mov_b64 s[16:17], s[76:77]\n\t"
      "s_mov_b64 s[20:21], s[78:79]\n\t"
      "s_mov_b64 s[24:25], s[80:81]\n\t"
      "s_mov_b64 s[12:13], s[74:75]\n\t"
      "s_mov_b64 s[8:9], s[72:73]\n\t"
      "s_mov_b64 s[28:29], s[82:83]\n\t"
      "s_mov_b64 s[36:37], s[84:85]\n\t"
      "s_lshl_b32 s48, s45, 2\n\t"
      "s_add_u32 s48, s48, s47\n\t"
      "s_lshl_b32 s48, s48, 7\n\t"
      "s_add_u32 s74, s48, 229376\n\t"
      "buffer_load_dwordx2 v[12:13], v115, s[40:43], s74 offen sc1\n\t"
      "buffer_load_dwordx2 v[96:97], v112, s[16:19], s63 offen\n\t"
      "buffer_load_dwordx2 v[98:99], v112, s[20:23], s63 offen\n\t"
      "buffer_load_dwordx4 v[100:103], v113, s[24:27], s64 offen\n\t"
      "buffer_load_dwordx4 v[32:35], v104, s[12:15], s59 offen nt\n\t"
      "buffer_load_dwordx4 v[36:39], v105, s[12:15], s59 offen nt\n\t"
      "buffer_load_dwordx4 v[40:43], v106, s[12:15], s59 offen nt\n\t"
      "buffer_load_dwordx4 v[44:47], v107, s[12:15], s59 offen nt\n\t"
      "buffer_load_dwordx4 v[48:51], v108, s[12:15], s59 offen nt\n\t"
      "buffer_load_dwordx4 v[52:55], v109, s[12:15], s59 offen nt\n\t"
      "buffer_load_dwordx4 v[56:59], v110, s[12:15], s59 offen nt\n\t"
      "buffer_load_dwordx4 v[60:63], v111, s[12:15], s59 offen nt\n\t"
      "s_mov_b32 s10, 4194304\n\t"
      "s_mov_b32 s11, 0x20000\n\t"
      "s_mov_b32 s30, 14680064\n\t"
      "s_mov_b32 s31, 0x20000\n\t"
      "s_mov_b32 s38, 7340032\n\t"
      "s_mov_b32 s39, 0x20000\n\t"
      "v_cmp_gt_u32 vcc, 8, v2\n\t"
      "v_lshlrev_b32 v11, 3, v2\n\t"
      "s_nop 0\n\t"
      "v_cndmask_b32 v11, 2.0, v11, vcc\n\t"
      "s_lshl_b32 s48, s45, 4\n\t"
      "s_add_u32 s48, s48, s56\n\t"
      "s_lshl_b32 s48, s48, 2\n\t"
      "s_add_u32 s48, s48, s47\n\t"
      "s_lshl_b32 s78, s48, 12\n\t"
      "s_lshl_b32 s48, s45, 2\n\t"
      "s_add_u32 s48, s48, s47\n\t"
      "s_lshl_b32 s48, s48, 4\n\t"
      "s_add_u32 s48, s48, s56\n\t"
      "s_lshl_b32 s79, s48, 7\n\t"
      "s_lshl_b32 s48, s47, 12\n\t"
      "s_add_u32 s48, s48, 81920\n\t"
      "v_add_u32 v117, s48, v10\n\t"
      "s_lshr_b32 s48, s47, 1\n\t"
      "s_and_b32 s49, s46, 1\n\t"
      "s_cmp_eq_u32 s48, s49\n\t"
      "s_cselect_b32 s48, 0, 0x40000000\n\t"
      "v_or_b32 v10, s48, v10\n\t"
      "v_or_b32 v11, s48, v11\n\t"
      "v_and_b32 v71, 15, v2\n\t"
      "v_lshrrev_b32 v72, 4, v2\n\t"
      "v_lshrrev_b32 v73, 1, v71\n\t"
      "v_xor_b32 v73, v73, v72\n\t"
      "s_lshl_b32 s48, s47, 13\n\t"
      "v_lshl_add_u32 v4, v71, 7, s48\n\t"
      "v_lshl_add_u32 v4, v73, 4, v4\n\t"
      "v_xor_b32 v5, 64, v4\n\t"
      "v_and_b32 v73, 1, v72\n\t"
      "v_mul_u32_u24 v73, 33, v73\n\t"
      "v_lshl_add_u32 v73, v71, 1, v73\n\t"
      "v_lshrrev_b32 v3, 1, v72\n\t"
      "v_lshl_add_u32 v73, v3, 8, v73\n\t"
      "v_lshlrev_b32 v73, 4, v73\n\t"
      "v_add_u32 v6, 32768, v73\n\t"
      "v_xor_b32 v7, 16, v6\n\t"
      "s_lshl_b32 s48, s46, 2\n\t"
      "s_add_u32 s48, s48, s47\n\t"
      "s_mul_i32 s48, s48, 458752\n\t"
      "s_lshl_b32 s49, s45, 8\n\t"
      "s_add_u32 s48, s48, s49\n\t"
      "v_mul_u32_u24 v73, 28672, v72\n\t"
      "v_lshl_add_u32 v73, v71, 4, v73\n\t"
      "v_add_u32 v8, s48, v73\n\t"
      "v_mov_b32 v128, 0\n\t"
      "v_mov_b32 v129, 0\n\t"
      "v_mov_b32 v130, 0\n\t"
      "v_mov_b32 v131, 0\n\t"
      "v_mov_b32 v132, 0\n\t"
      "v_mov_b32 v133, 0\n\t"
      "v_mov_b32 v134, 0\n\t"
      "v_mov_b32 v135, 0\n\t"
      "v_mov_b32 v136, 0\n\t"
      "v_mov_b32 v137, 0\n\t"
      "v_mov_b32 v138, 0\n\t"
      "v_mov_b32 v139, 0\n\t"
      "v_mov_b32 v140, 0\n\t"
      "v_mov_b32 v141, 0\n\t"
      "v_mov_b32 v142, 0\n\t"
      "v_mov_b32 v143, 0\n\t"
      "v_mov_b32 v144, 0\n\t"
      "v_mov_b32 v145, 0\n\t"
      "v_mov_b32 v146, 0\n\t"
      "v_mov_b32 v147, 0\n\t"
      "v_mov_b32 v148, 0\n\t"
      "v_mov_b32 v149, 0\n\t"
      "v_mov_b32 v150, 0\n\t"
      "v_mov_b32 v151, 0\n\t"
      "v_mov_b32 v152, 0\n\t"
      "v_mov_b32 v153, 0\n\t"
      "v_mov_b32 v154, 0\n\t"
      "v_mov_b32 v155, 0\n\t"
      "v_mov_b32 v156, 0\n\t"
      "v_mov_b32 v157, 0\n\t"
      "v_mov_b32 v158, 0\n\t"
      "v_mov_b32 v159, 0\n\t"
      "v_mov_b32 v160, 0\n\t"
      "v_mov_b32 v161, 0\n\t"
      "v_mov_b32 v162, 0\n\t"
      "v_mov_b32 v163, 0\n\t"
      "v_mov_b32 v164, 0\n\t"
      "v_mov_b32 v165, 0\n\t"
      "v_mov_b32 v166, 0\n\t"
      "v_mov_b32 v167, 0\n\t"
      "v_mov_b32 v168, 0\n\t"
      "v_mov_b32 v169, 0\n\t"
      "v_mov_b32 v170, 0\n\t"
      "v_mov_b32 v171, 0\n\t"
      "v_mov_b32 v172, 0\n\t"
      "v_mov_b32 v173, 0\n\t"
      "v_mov_b32 v174, 0\n\t"
      "v_mov_b32 v175, 0\n\t"
      "v_mov_b32 v176, 0\n\t"
      "v_mov_b32 v177, 0\n\t"
      "v_mov_b32 v178, 0\n\t"
      "v_mov_b32 v179, 0\n\t"
      "v_mov_b32 v180, 0\n\t"
      "v_mov_b32 v181, 0\n\t"
      "v_mov_b32 v182, 0\n\t"
      "v_mov_b32 v183, 0\n\t"
      "v_mov_b32 v184, 0\n\t"
      "v_mov_b32 v185, 0\n\t"
      "v_mov_b32 v186, 0\n\t"
      "v_mov_b32 v187, 0\n\t"
      "v_mov_b32 v188, 0\n\t"
      "v_mov_b32 v189, 0\n\t"
      "v_mov_b32 v190, 0\n\t"
      "v_mov_b32 v191, 0\n\t"
      "v_mov_b32 v192, 0\n\t"
      "v_mov_b32 v193, 0\n\t"
      "v_mov_b32 v194, 0\n\t"
      "v_mov_b32 v195, 0\n\t"
      "v_mov_b32 v196, 0\n\t"
      "v_mov_b32 v197, 0\n\t"
      "v_mov_b32 v198, 0\n\t"
      "v_mov_b32 v199, 0\n\t"
      "v_mov_b32 v200, 0\n\t"
      "v_mov_b32 v201, 0\n\t"
      "v_mov_b32 v202, 0\n\t"
      "v_mov_b32 v203, 0\n\t"
      "v_mov_b32 v204, 0\n\t"
      "v_mov_b32 v205, 0\n\t"
      "v_mov_b32 v206, 0\n\t"
      "v_mov_b32 v207, 0\n\t"
      "v_mov_b32 v208, 0\n\t"
      "v_mov_b32 v209, 0\n\t"
      "v_mov_b32 v210, 0\n\t"
      "v_mov_b32 v211, 0\n\t"
      "v_mov_b32 v212, 0\n\t"
      "v_mov_b32 v213, 0\n\t"
      "v_mov_b32 v214, 0\n\t"
      "v_mov_b32 v215, 0\n\t"
      "v_mov_b32 v216, 0\n\t"
      "v_mov_b32 v217, 0\n\t"
      "v_mov_b32 v218, 0\n\t"
      "v_mov_b32 v219, 0\n\t"
      "v_mov_b32 v220, 0\n\t"
      "v_mov_b32 v221, 0\n\t"
      "v_mov_b32 v222, 0\n\t"
      "v_mov_b32 v223, 0\n\t"
      "v_mov_b32 v224, 0\n\t"
      "v_mov_b32 v225, 0\n\t"
      "v_mov_b32 v226, 0\n\t"
      "v_mov_b32 v227, 0\n\t"
      "v_mov_b32 v228, 0\n\t"
      "v_mov_b32 v229, 0\n\t"
      "v_mov_b32 v230, 0\n\t"
      "v_mov_b32 v231, 0\n\t"
      "v_mov_b32 v232, 0\n\t"
      "v_mov_b32 v233, 0\n\t"
      "v_mov_b32 v234, 0\n\t"
      "v_mov_b32 v235, 0\n\t"
      "v_mov_b32 v236, 0\n\t"
      "v_mov_b32 v237, 0\n\t"
      "v_mov_b32 v238, 0\n\t"
      "v_mov_b32 v239, 0\n\t"
      "v_mov_b32 v240, 0\n\t"
      "v_mov_b32 v241, 0\n\t"
      "v_mov_b32 v242, 0\n\t"
      "v_mov_b32 v243, 0\n\t"
      "v_mov_b32 v244, 0\n\t"
      "v_mov_b32 v245, 0\n\t"
      "v_mov_b32 v246, 0\n\t"
      "v_mov_b32 v247, 0\n\t"
      "v_mov_b32 v248, 0\n\t"
      "v_mov_b32 v249, 0\n\t"
      "v_mov_b32 v250, 0\n\t"
      "v_mov_b32 v251, 0\n\t"
      "v_mov_b32 v252, 0\n\t"
      "v_mov_b32 v253, 0\n\t"
      "v_mov_b32 v254, 0\n\t"
      "v_mov_b32 v255, 0\n\t"
      "s_mov_b32 s75, 1\n\t"
      "s_mov_b32 s57, 0\n\t"
      "s_mov_b32 s76, 49152\n\t"
      "s_waitcnt vmcnt(8)\n\t"
      "v_lshlrev_b32 v80, 16, v96\n\t"
      "v_and_b32 v81, 0xffff0000, v96\n\t"
      "v_lshlrev_b32 v84, 16, v98\n\t"
      "v_and_b32 v85, 0xffff0000, v98\n\t"
      "v_lshlrev_b32 v82, 16, v97\n\t"
      "v_and_b32 v83, 0xffff0000, v97\n\t"
      "v_lshlrev_b32 v86, 16, v99\n\t"
      "v_and_b32 v87, 0xffff0000, v99\n\t"
      "v_lshlrev_b32 v88, 16, v100\n\t"
      "v_and_b32 v89, 0xffff0000, v100\n\t"
      "v_lshlrev_b32 v90, 16, v101\n\t"
      "v_and_b32 v91, 0xffff0000, v101\n\t"
      "v_lshlrev_b32 v92, 16, v102\n\t"
      "v_and_b32 v93, 0xffff0000, v102\n\t"
      "v_lshlrev_b32 v94, 16, v103\n\t"
      "v_and_b32 v95, 0xffff0000, v103\n\t"
      "v_mul_f32 v84, -v84, v80\n\t"
      "v_mul_f32 v85, -v85, v81\n\t"
      "v_mul_f32 v86, -v86, v82\n\t"
      "v_mul_f32 v87, -v87, v83\n\t"
      "s_waitcnt vmcnt(6)\n\t"
      "v_cvt_f32_i32 v32, v32\n\t"
      "v_cvt_f32_i32 v36, v36\n\t"
      "v_cvt_f32_i32 v33, v33\n\t"
      "v_cvt_f32_i32 v37, v37\n\t"
      "v_cvt_f32_i32 v34, v34\n\t"
      "v_cvt_f32_i32 v38, v38\n\t"
      "v_cvt_f32_i32 v35, v35\n\t"
      "v_cvt_f32_i32 v39, v39\n\t"
      "v_fma_f32 v32, v32, v80, v84\n\t"
      "v_fma_f32 v36, v36, v80, v84\n\t"
      "v_fma_f32 v33, v33, v81, v85\n\t"
      "v_fma_f32 v37, v37, v81, v85\n\t"
      "v_fma_f32 v34, v34, v82, v86\n\t"
      "v_fma_f32 v38, v38, v82, v86\n\t"
      "v_fma_f32 v35, v35, v83, v87\n\t"
      "v_fma_f32 v39, v39, v83, v87\n\t"
      "v_mul_f32 v32, v32, v88\n\t"
      "v_mul_f32 v36, v36, v89\n\t"
      "v_mul_f32 v33, v33, v88\n\t"
      "v_mul_f32 v37, v37, v89\n\t"
      "v_mul_f32 v34, v34, v88\n\t"
      "v_mul_f32 v38, v38, v89\n\t"
      "v_mul_f32 v35, v35, v88\n\t"
      "v_mul_f32 v39, v39, v89\n\t"
      "v_cvt_pk_bf16_f32 v16, v32, v36\n\t"
      "v_cvt_pk_bf16_f32 v20, v33, v37\n\t"
      "v_cvt_pk_bf16_f32 v24, v34, v38\n\t"
      "v_cvt_pk_bf16_f32 v28, v35, v39\n\t"
      "s_waitcnt vmcnt(4)\n\t"
      "v_cvt_f32_i32 v40, v40\n\t"
      "v_cvt_f32_i32 v44, v44\n\t"
      "v_cvt_f32_i32 v41, v41\n\t"
      "v_cvt_f32_i32 v45, v45\n\t"
      "v_cvt_f32_i32 v42, v42\n\t"
      "v_cvt_f32_i32 v46, v46\n\t"
      "v_cvt_f32_i32 v43, v43\n\t"
      "v_cvt_f32_i32 v47, v47\n\t"
      "v_fma_f32 v40, v40, v80, v84\n\t"
      "v_fma_f32 v44, v44, v80, v84\n\t"
      "v_fma_f32 v41, v41, v81, v85\n\t"
      "v_fma_f32 v45, v45, v81, v85\n\t"
      "v_fma_f32 v42, v42, v82, v86\n\t"
      "v_fma_f32 v46, v46, v82, v86\n\t"
      "v_fma_f32 v43, v43, v83, v87\n\t"
      "v_fma_f32 v47, v47, v83, v87\n\t"
      "v_mul_f32 v40, v40, v90\n\t"
      "v_mul_f32 v44, v44, v91\n\t"
      "v_mul_f32 v41, v41, v90\n\t"
      "v_mul_f32 v45, v45, v91\n\t"
      "v_mul_f32 v42, v42, v90\n\t"
      "v_mul_f32 v46, v46, v91\n\t"
      "v_mul_f32 v43, v43, v90\n\t"
      "v_mul_f32 v47, v47, v91\n\t"
      "v_cvt_pk_bf16_f32 v17, v40, v44\n\t"
      "v_cvt_pk_bf16_f32 v21, v41, v45\n\t"
      "v_cvt_pk_bf16_f32 v25, v42, v46\n\t"
      "v_cvt_pk_bf16_f32 v29, v43, v47\n\t"
      "s_waitcnt vmcnt(2)\n\t"
      "v_cvt_f32_i32 v48, v48\n\t"
      "v_cvt_f32_i32 v52, v52\n\t"
      "v_cvt_f32_i32 v49, v49\n\t"
      "v_cvt_f32_i32 v53, v53\n\t"
      "v_cvt_f32_i32 v50, v50\n\t"
      "v_cvt_f32_i32 v54, v54\n\t"
      "v_cvt_f32_i32 v51, v51\n\t"
      "v_cvt_f32_i32 v55, v55\n\t"
      "v_fma_f32 v48, v48, v80, v84\n\t"
      "v_fma_f32 v52, v52, v80, v84\n\t"
      "v_fma_f32 v49, v49, v81, v85\n\t"
      "v_fma_f32 v53, v53, v81, v85\n\t"
      "v_fma_f32 v50, v50, v82, v86\n\t"
      "v_fma_f32 v54, v54, v82, v86\n\t"
      "v_fma_f32 v51, v51, v83, v87\n\t"
      "v_fma_f32 v55, v55, v83, v87\n\t"
      "v_mul_f32 v48, v48, v92\n\t"
      "v_mul_f32 v52, v52, v93\n\t"
      "v_mul_f32 v49, v49, v92\n\t"
      "v_mul_f32 v53, v53, v93\n\t"
      "v_mul_f32 v50, v50, v92\n\t"
      "v_mul_f32 v54, v54, v93\n\t"
      "v_mul_f32 v51, v51, v92\n\t"
      "v_mul_f32 v55, v55, v93\n\t"
      "v_cvt_pk_bf16_f32 v18, v48, v52\n\t"
      "v_cvt_pk_bf16_f32 v22, v49, v53\n\t"
      "v_cvt_pk_bf16_f32 v26, v50, v54\n\t"
      "v_cvt_pk_bf16_f32 v30, v51, v55\n\t"
      "s_waitcnt vmcnt(0)\n\t"
      "v_cvt_f32_i32 v56, v56\n\t"
      "v_cvt_f32_i32 v60, v60\n\t"
      "v_cvt_f32_i32 v57, v57\n\t"
      "v_cvt_f32_i32 v61, v61\n\t"
      "v_cvt_f32_i32 v58, v58\n\t"
      "v_cvt_f32_i32 v62, v62\n\t"
      "v_cvt_f32_i32 v59, v59\n\t"
      "v_cvt_f32_i32 v63, v63\n\t"
      "v_fma_f32 v56, v56, v80, v84\n\t"
      "v_fma_f32 v60, v60, v80, v84\n\t"
      "v_fma_f32 v57, v57, v81, v85\n\t"
      "v_fma_f32 v61, v61, v81, v85\n\t"
      "v_fma_f32 v58, v58, v82, v86\n\t"
      "v_fma_f32 v62, v62, v82, v86\n\t"
      "v_fma_f32 v59, v59, v83, v87\n\t"
      "v_fma_f32 v63, v63, v83, v87\n\t"
      "v_mul_f32 v56, v56, v94\n\t"
      "v_mul_f32 v60, v60, v95\n\t"
      "v_mul_f32 v57, v57, v94\n\t"
      "v_mul_f32 v61, v61, v95\n\t"
      "v_mul_f32 v58, v58, v94\n\t"
      "v_mul_f32 v62, v62, v95\n\t"
      "v_mul_f32 v59, v59, v94\n\t"
      "v_mul_f32 v63, v63, v95\n\t"
      "v_cvt_pk_bf16_f32 v19, v56, v60\n\t"
      "ds_write_b128 v117, v[16:19] offset:0\n\t"
      "v_cvt_pk_bf16_f32 v23, v57, v61\n\t"
      "ds_write_b128 v117, v[20:23] offset:1024\n\t"
      "v_cvt_pk_bf16_f32 v27, v58, v62\n\t"
      "ds_write_b128 v117, v[24:27] offset:2048\n\t"
      "v_cvt_pk_bf16_f32 v31, v59, v63\n\t"
      "ds_write_b128 v117, v[28:31] offset:3072\n\t"
      "buffer_store_dwordx4 v[16:19], v10, s[36:39], s78 offen offset:0 sc1\n\t"
      "buffer_store_dwordx4 v[20:23], v10, s[36:39], s78 offen offset:1024 sc1\n\t"
      "buffer_store_dwordx4 v[24:27], v10, s[36:39], s78 offen offset:2048 sc1\n\t"
      "buffer_store_dwordx4 v[28:31], v10, s[36:39], s78 offen offset:3072 sc1\n\t"
      "s_waitcnt lgkmcnt(0)\n\t"
      "s_barrier\n\t"
      "ds_read_b128 v[32:35], v4 offset:0\n\t"
      "ds_read_b128 v[48:51], v6 offset:0\n\t"
      "ds_read_b128 v[80:83], v5 offset:0\n\t"
      "ds_read_b128 v[52:55], v6 offset:8192\n\t"
      "ds_read_b128 v[56:59], v6 offset:1024\n\t"
      "ds_read_b128 v[60:63], v6 offset:9216\n\t"
      "ds_read_b128 v[64:67], v6 offset:2048\n\t"
      "ds_read_b128 v[68:71], v6 offset:10240\n\t"
      "ds_read_b128 v[72:75], v6 offset:3072\n\t"
      "ds_read_b128 v[76:79], v6 offset:11264\n\t"
      "ds_read_b128 v[84:87], v5 offset:2048\n\t"
      "ds_read_b128 v[36:39], v4 offset:2048\n\t"
      "s_waitcnt lgkmcnt(2)\n\t"
      "v_mfma_f32_16x16x32_bf16 v[128:131], v[32:35], v[48:51], v[128:131]\n\t"
      "v_mfma_f32_16x16x32_bf16 v[128:131], v[80:83], v[52:55], v[128:131]\n\t"
      "ds_read_b128 v[40:43], v4 offset:4096\n\t"
      "v_mfma_f32_16x16x32_bf16 v[132:135], v[32:35], v[56:59], v[132:135]\n\t"
      "v_mfma_f32_16x16x32_bf16 v[132:135], v[80:83], v[60:63], v[132:135]\n\t"
      "ds_read_b128 v[88:91], v5 offset:4096\n\t"
      "v_mfma_f32_16x16x32_bf16 v[136:139], v[32:35], v[64:67], v[136:139]\n\t"
      "v_mfma_f32_16x16x32_bf16 v[136:139], v[80:83], v[68:71], v[136:139]\n\t"
      "ds_read_b128 v[92:95], v5 offset:6144\n\t"
      "v_mfma_f32_16x16x32_bf16 v[140:143], v[32:35], v[72:75], v[140:143]\n\t"
      "v_mfma_f32_16x16x32_bf16 v[140:143], v[80:83], v[76:79], v[140:143]\n\t"
      "ds_read_b128 v[44:47], v4 offset:6144\n\t"
      "s_waitcnt lgkmcnt(4)\n\t"
      "v_mfma_f32_16x16x32_bf16 v[172:175], v[84:87], v[76:79], v[172:175]\n\t"
      "v_mfma_f32_16x16x32_bf16 v[172:175], v[36:39], v[72:75], v[172:175]\n\t"
      "ds_read_b128 v[96:99], v7 offset:0\n\t"
      "v_mfma_f32_16x16x32_bf16 v[168:171], v[84:87], v[68:71], v[168:171]\n\t"
      "v_mfma_f32_16x16x32_bf16 v[168:171], v[36:39], v[64:67], v[168:171]\n\t"
      "ds_read_b128 v[100:103], v7 offset:8192\n\t"
      "v_mfma_f32_16x16x32_bf16 v[164:167], v[84:87], v[60:63], v[164:167]\n\t"
      "v_mfma_f32_16x16x32_bf16 v[164:167], v[36:39], v[56:59], v[164:167]\n\t"
      "ds_read_b128 v[104:107], v7 offset:1024\n\t"
      "v_mfma_f32_16x16x32_bf16 v[160:163], v[84:87], v[52:55], v[160:163]\n\t"
      "v_mfma_f32_16x16x32_bf16 v[160:163], v[36:39], v[48:51], v[160:163]\n\t"
      "ds_read_b128 v[108:111], v7 offset:9216\n\t"
      "s_waitcnt lgkmcnt(4)\n\t"
      "v_mfma_f32_16x16x32_bf16 v[192:195], v[40:43], v[48:51], v[192:195]\n\t"
      "v_mfma_f32_16x16x32_bf16 v[192:195], v[88:91], v[52:55], v[192:195]\n\t"
      "ds_read_b128 v[112:115], v7 offset:2048\n\t"
      "v_mfma_f32_16x16x32_bf16 v[196:199], v[40:43], v[56:59], v[196:199]\n\t"
      "v_mfma_f32_16x16x32_bf16 v[196:199], v[88:91], v[60:63], v[196:199]\n\t"
      "ds_read_b128 v[116:119], v7 offset:10240\n\t"
      "v_mfma_f32_16x16x32_bf16 v[200:203], v[40:43], v[64:67], v[200:203]\n\t"
      "v_mfma_f32_16x16x32_bf16 v[200:203], v[88:91], v[68:71], v[200:203]\n\t"
      "ds_read_b128 v[120:123], v7 offset:3072\n\t"
      "v_mfma_f32_16x16x32_bf16 v[204:207], v[40:43], v[72:75], v[204:207]\n\t"
      "v_mfma_f32_16x16x32_bf16 v[204:207], v[88:91], v[76:79], v[204:207]\n\t"
      "ds_read_b128 v[124:127], v7 offset:11264\n\t"
      "v_mfma_f32_16x16x32_bf16 v[236:239], v[92:95], v[76:79], v[236:239]\n\t"
      "v_mfma_f32_16x16x32_bf16 v[236:239], v[44:47], v[72:75], v[236:239]\n\t"
      "s_add_u32 s57, s57, 1\n\t"
      "s_cmp_eq_u32 s57, 3\n\t"
      "v_mfma_f32_16x16x32_bf16 v[232:235], v[92:95], v[68:71], v[232:235]\n\t"
      "s_cselect_b32 s58, 0xfffe8000, s76\n\t"
      "s_cselect_b32 s57, 0, s57\n\t"
      "v_mfma_f32_16x16x32_bf16 v[232:235], v[44:47], v[64:67], v[232:235]\n\t"
      "v_add_u32 v4, s58, v4\n\t"
      "v_mfma_f32_16x16x32_bf16 v[228:231], v[92:95], v[60:63], v[228:231]\n\t"
      "v_add_u32 v5, s58, v5\n\t"
      "v_mfma_f32_16x16x32_bf16 v[228:231], v[44:47], v[56:59], v[228:231]\n\t"
      "v_add_u32 v6, s58, v6\n\t"
      "v_mfma_f32_16x16x32_bf16 v[224:227], v[92:95], v[52:55], v[224:227]\n\t"
      "v_add_u32 v7, s58, v7\n\t"
      "v_mfma_f32_16x16x32_bf16 v[224:227], v[44:47], v[48:51], v[224:227]\n\t"
      "s_waitcnt lgkmcnt(0)\n\t"
      "v_mfma_f32_16x16x32_bf16 v[144:147], v[32:35], v[96:99], v[144:147]\n\t"
      "v_mfma_f32_16x16x32_bf16 v[144:147], v[80:83], v[100:103], v[144:147]\n\t"
      "v_mfma_f32_16x16x32_bf16 v[148:151], v[32:35], v[104:107], v[148:151]\n\t"
      "v_mfma_f32_16x16x32_bf16 v[148:151], v[80:83], v[108:111], v[148:151]\n\t"
      "v_mfma_f32_16x16x32_bf16 v[152:155], v[32:35], v[112:115], v[152:155]\n\t"
      "v_mfma_f32_16x16x32_bf16 v[152:155], v[80:83], v[116:119], v[152:155]\n\t"
      "v_mfma_f32_16x16x32_bf16 v[156:159], v[32:35], v[120:123], v[156:159]\n\t"
      "v_mfma_f32_16x16x32_bf16 v[156:159], v[80:83], v[124:127], v[156:159]\n\t"
      "v_mfma_f32_16x16x32_bf16 v[188:191], v[84:87], v[124:127], v[188:191]\n\t"
      "v_mfma_f32_16x16x32_bf16 v[188:191], v[36:39], v[120:123], v[188:191]\n\t"
      "v_mfma_f32_16x16x32_bf16 v[184:187], v[84:87], v[116:119], v[184:187]\n\t"
      "v_mfma_f32_16x16x32_bf16 v[184:187], v[36:39], v[112:115], v[184:187]\n\t"
      "s_waitcnt vmcnt(0)\n\t"
      "v_add_u32 v12, 1, v12\n\t"
      "buffer_store_dwordx2 v[12:13], v11, s[40:43], s79 offen sc1\n\t"
      "v_mfma_f32_16x16x32_bf16 v[180:183], v[84:87], v[108:111], v[180:183]\n\t"
      "v_mfma_f32_16x16x32_bf16 v[180:183], v[36:39], v[104:107], v[180:183]\n\t"
      "v_mfma_f32_16x16x32_bf16 v[176:179], v[84:87], v[100:103], v[176:179]\n\t"
      "v_mfma_f32_16x16x32_bf16 v[176:179], v[36:39], v[96:99], v[176:179]\n\t"
      "v_mfma_f32_16x16x32_bf16 v[208:211], v[40:43], v[96:99], v[208:211]\n\t"
      "v_mfma_f32_16x16x32_bf16 v[208:211], v[88:91], v[100:103], v[208:211]\n\t"
      "s_barrier\n\t"
      "ds_read_b128 v[32:35], v4 offset:0\n\t"
      "v_mfma_f32_16x16x32_bf16 v[212:215], v[40:43], v[104:107], v[212:215]\n\t"
      "ds_read_b128 v[48:51], v6 offset:0\n\t"
      "v_mfma_f32_16x16x32_bf16 v[212:215], v[88:91], v[108:111], v[212:215]\n\t"
      "ds_read_b128 v[80:83], v5 offset:0\n\t"
      "v_mfma_f32_16x16x32_bf16 v[216:219], v[40:43], v[112:115], v[216:219]\n\t"
      "ds_read_b128 v[52:55], v6 offset:8192\n\t"
      "v_mfma_f32_16x16x32_bf16 v[216:219], v[88:91], v[116:119], v[216:219]\n\t"
      "ds_read_b128 v[56:59], v6 offset:1024\n\t"
      "v_mfma_f32_16x16x32_bf16 v[220:223], v[40:43], v[120:123], v[220:223]\n\t"
      "ds_read_b128 v[60:63], v6 offset:9216\n\t"
      "v_mfma_f32_16x16x32_bf16 v[220:223], v[88:91], v[124:127], v[220:223]\n\t"
      "ds_read_b128 v[64:67], v6 offset:2048\n\t"
      "v_mfma_f32_16x16x32_bf16 v[252:255], v[92:95], v[124:127], v[252:255]\n\t"
      "ds_read_b128 v[68:71], v6 offset:10240\n\t"
      "v_mfma_f32_16x16x32_bf16 v[252:255], v[44:47], v[120:123], v[252:255]\n\t"
      "ds_read_b128 v[72:75], v6 offset:3072\n\t"
      "v_mfma_f32_16x16x32_bf16 v[248:251], v[92:95], v[116:119], v[248:251]\n\t"
      "ds_read_b128 v[76:79], v6 offset:11264\n\t"
      "v_mfma_f32_16x16x32_bf16 v[248:251], v[44:47], v[112:115], v[248:251]\n\t"
      "ds_read_b128 v[84:87], v5 offset:2048\n\t"
      "v_mfma_f32_16x16x32_bf16 v[244:247], v[92:95], v[108:111], v[244:247]\n\t"
      "ds_read_b128 v[36:39], v4 offset:2048\n\t"
      "v_mfma_f32_16x16x32_bf16 v[244:247], v[44:47], v[104:107], v[244:247]\n\t"
      "v_mfma_f32_16x16x32_bf16 v[240:243], v[92:95], v[100:103], v[240:243]\n\t"
      "v_mfma_f32_16x16x32_bf16 v[240:243], v[44:47], v[96:99], v[240:243]\n\t"
      ".Lc_loop%=:\n\t"
      "s_waitcnt lgkmcnt(2)\n\t"
      "v_mfma_f32_16x16x32_bf16 v[128:131], v[32:35], v[48:51], v[128:131]\n\t"
      "v_mfma_f32_16x16x32_bf16 v[128:131], v[80:83], v[52:55], v[128:131]\n\t"
      "ds_read_b128 v[40:43], v4 offset:4096\n\t"
      "v_mfma_f32_16x16x32_bf16 v[132:135], v[32:35], v[56:59], v[132:135]\n\t"
      "v_mfma_f32_16x16x32_bf16 v[132:135], v[80:83], v[60:63], v[132:135]\n\t"
      "ds_read_b128 v[88:91], v5 offset:4096\n\t"
      "v_mfma_f32_16x16x32_bf16 v[136:139], v[32:35], v[64:67], v[136:139]\n\t"
      "v_mfma_f32_16x16x32_bf16 v[136:139], v[80:83], v[68:71], v[136:139]\n\t"
      "ds_read_b128 v[92:95], v5 offset:6144\n\t"
      "v_mfma_f32_16x16x32_bf16 v[140:143], v[32:35], v[72:75], v[140:143]\n\t"
      "v_mfma_f32_16x16x32_bf16 v[140:143], v[80:83], v[76:79], v[140:143]\n\t"
      "ds_read_b128 v[44:47], v4 offset:6144\n\t"
      "s_waitcnt lgkmcnt(4)\n\t"
      "v_mfma_f32_16x16x32_bf16 v[172:175], v[84:87], v[76:79], v[172:175]\n\t"
      "v_mfma_f32_16x16x32_bf16 v[172:175], v[36:39], v[72:75], v[172:175]\n\t"
      "ds_read_b128 v[96:99], v7 offset:0\n\t"
      "v_mfma_f32_16x16x32_bf16 v[168:171], v[84:87], v[68:71], v[168:171]\n\t"
      "v_mfma_f32_16x16x32_bf16 v[168:171], v[36:39], v[64:67], v[168:171]\n\t"
      "ds_read_b128 v[100:103], v7 offset:8192\n\t"
      "v_mfma_f32_16x16x32_bf16 v[164:167], v[84:87], v[60:63], v[164:167]\n\t"
      "v_mfma_f32_16x16x32_bf16 v[164:167], v[36:39], v[56:59], v[164:167]\n\t"
      "ds_read_b128 v[104:107], v7 offset:1024\n\t"
      "v_mfma_f32_16x16x32_bf16 v[160:163], v[84:87], v[52:55], v[160:163]\n\t"
      "v_mfma_f32_16x16x32_bf16 v[160:163], v[36:39], v[48:51], v[160:163]\n\t"
      "ds_read_b128 v[108:111], v7 offset:9216\n\t"
      "s_waitcnt lgkmcnt(4)\n\t"
      "v_mfma_f32_16x16x32_bf16 v[192:195], v[40:43], v[48:51], v[192:195]\n\t"
      "v_mfma_f32_16x16x32_bf16 v[192:195], v[88:91], v[52:55], v[192:195]\n\t"
      "ds_read_b128 v[112:115], v7 offset:2048\n\t"
      "v_mfma_f32_16x16x32_bf16 v[196:199], v[40:43], v[56:59], v[196:199]\n\t"
      "v_mfma_f32_16x16x32_bf16 v[196:199], v[88:91], v[60:63], v[196:199]\n\t"
      "ds_read_b128 v[116:119], v7 offset:10240\n\t"
      "v_mfma_f32_16x16x32_bf16 v[200:203], v[40:43], v[64:67], v[200:203]\n\t"
      "v_mfma_f32_16x16x32_bf16 v[200:203], v[88:91], v[68:71], v[200:203]\n\t"
      "ds_read_b128 v[120:123], v7 offset:3072\n\t"
      "v_mfma_f32_16x16x32_bf16 v[204:207], v[40:43], v[72:75], v[204:207]\n\t"
      "v_mfma_f32_16x16x32_bf16 v[204:207], v[88:91], v[76:79], v[204:207]\n\t"
      "ds_read_b128 v[124:127], v7 offset:11264\n\t"
      "v_mfma_f32_16x16x32_bf16 v[236:239], v[92:95], v[76:79], v[236:239]\n\t"
      "v_mfma_f32_16x16x32_bf16 v[236:239], v[44:47], v[72:75], v[236:239]\n\t"
      "s_add_u32 s57, s57, 1\n\t"
      "s_cmp_eq_u32 s57, 3\n\t"
      "v_mfma_f32_16x16x32_bf16 v[232:235], v[92:95], v[68:71], v[232:235]\n\t"
      "s_cselect_b32 s58, 0xfffe8000, s76\n\t"
      "s_cselect_b32 s57, 0, s57\n\t"
      "v_mfma_f32_16x16x32_bf16 v[232:235], v[44:47], v[64:67], v[232:235]\n\t"
      "v_add_u32 v4, s58, v4\n\t"
      "v_mfma_f32_16x16x32_bf16 v[228:231], v[92:95], v[60:63], v[228:231]\n\t"
      "v_add_u32 v5, s58, v5\n\t"
      "v_mfma_f32_16x16x32_bf16 v[228:231], v[44:47], v[56:59], v[228:231]\n\t"
      "v_add_u32 v6, s58, v6\n\t"
      "v_mfma_f32_16x16x32_bf16 v[224:227], v[92:95], v[52:55], v[224:227]\n\t"
      "v_add_u32 v7, s58, v7\n\t"
      "v_mfma_f32_16x16x32_bf16 v[224:227], v[44:47], v[48:51], v[224:227]\n\t"
      "s_waitcnt lgkmcnt(0)\n\t"
      "v_mfma_f32_16x16x32_bf16 v[144:147], v[32:35], v[96:99], v[144:147]\n\t"
      "v_mfma_f32_16x16x32_bf16 v[144:147], v[80:83], v[100:103], v[144:147]\n\t"
      "v_mfma_f32_16x16x32_bf16 v[148:151], v[32:35], v[104:107], v[148:151]\n\t"
      "v_mfma_f32_16x16x32_bf16 v[148:151], v[80:83], v[108:111], v[148:151]\n\t"
      "v_mfma_f32_16x16x32_bf16 v[152:155], v[32:35], v[112:115], v[152:155]\n\t"
      "v_mfma_f32_16x16x32_bf16 v[152:155], v[80:83], v[116:119], v[152:155]\n\t"
      "v_mfma_f32_16x16x32_bf16 v[156:159], v[32:35], v[120:123], v[156:159]\n\t"
      "v_mfma_f32_16x16x32_bf16 v[156:159], v[80:83], v[124:127], v[156:159]\n\t"
      "v_mfma_f32_16x16x32_bf16 v[188:191], v[84:87], v[124:127], v[188:191]\n\t"
      "v_mfma_f32_16x16x32_bf16 v[188:191], v[36:39], v[120:123], v[188:191]\n\t"
      "v_mfma_f32_16x16x32_bf16 v[184:187], v[84:87], v[116:119], v[184:187]\n\t"
      "v_mfma_f32_16x16x32_bf16 v[184:187], v[36:39], v[112:115], v[184:187]\n\t"
      "v_mfma_f32_16x16x32_bf16 v[180:183], v[84:87], v[108:111], v[180:183]\n\t"
      "v_mfma_f32_16x16x32_bf16 v[180:183], v[36:39], v[104:107], v[180:183]\n\t"
      "s_add_u32 s75, s75, 1\n\t"
      "v_mfma_f32_16x16x32_bf16 v[176:179], v[84:87], v[100:103], v[176:179]\n\t"
      "v_mfma_f32_16x16x32_bf16 v[176:179], v[36:39], v[96:99], v[176:179]\n\t"
      "v_mfma_f32_16x16x32_bf16 v[208:211], v[40:43], v[96:99], v[208:211]\n\t"
      "v_mfma_f32_16x16x32_bf16 v[208:211], v[88:91], v[100:103], v[208:211]\n\t"
      "s_barrier\n\t"
      "ds_read_b128 v[32:35], v4 offset:0\n\t"
      "v_mfma_f32_16x16x32_bf16 v[212:215], v[40:43], v[104:107], v[212:215]\n\t"
      "ds_read_b128 v[48:51], v6 offset:0\n\t"
      "v_mfma_f32_16x16x32_bf16 v[212:215], v[88:91], v[108:111], v[212:215]\n\t"
      "ds_read_b128 v[80:83], v5 offset:0\n\t"
      "v_mfma_f32_16x16x32_bf16 v[216:219], v[40:43], v[112:115], v[216:219]\n\t"
      "ds_read_b128 v[52:55], v6 offset:8192\n\t"
      "v_mfma_f32_16x16x32_bf16 v[216:219], v[88:91], v[116:119], v[216:219]\n\t"
      "ds_read_b128 v[56:59], v6 offset:1024\n\t"
      "v_mfma_f32_16x16x32_bf16 v[220:223], v[40:43], v[120:123], v[220:223]\n\t"
      "ds_read_b128 v[60:63], v6 offset:9216\n\t"
      "v_mfma_f32_16x16x32_bf16 v[220:223], v[88:91], v[124:127], v[220:223]\n\t"
      "ds_read_b128 v[64:67], v6 offset:2048\n\t"
      "v_mfma_f32_16x16x32_bf16 v[252:255], v[92:95], v[124:127], v[252:255]\n\t"
      "ds_read_b128 v[68:71], v6 offset:10240\n\t"
      "v_mfma_f32_16x16x32_bf16 v[252:255], v[44:47], v[120:123], v[252:255]\n\t"
      "ds_read_b128 v[72:75], v6 offset:3072\n\t"
      "v_mfma_f32_16x16x32_bf16 v[248:251], v[92:95], v[116:119], v[248:251]\n\t"
      "ds_read_b128 v[76:79], v6 offset:11264\n\t"
      "v_mfma_f32_16x16x32_bf16 v[248:251], v[44:47], v[112:115], v[248:251]\n\t"
      "ds_read_b128 v[84:87], v5 offset:2048\n\t"
      "v_mfma_f32_16x16x32_bf16 v[244:247], v[92:95], v[108:111], v[244:247]\n\t"
      "ds_read_b128 v[36:39], v4 offset:2048\n\t"
      "v_mfma_f32_16x16x32_bf16 v[244:247], v[44:47], v[104:107], v[244:247]\n\t"
      "v_mfma_f32_16x16x32_bf16 v[240:243], v[92:95], v[100:103], v[240:243]\n\t"
      "s_cmp_lg_u32 s75, 14\n\t"
      "v_mfma_f32_16x16x32_bf16 v[240:243], v[44:47], v[96:99], v[240:243]\n\t"
      "s_cbranch_scc1 .Lc_loop%=\n\t"
      ".Lc_tail%=:\n\t"
      "s_waitcnt lgkmcnt(10)\n\t"
      "v_mfma_f32_16x16x32_bf16 v[128:131], v[32:35], v[48:51], v[128:131]\n\t"
      "ds_read_b128 v[96:99], v7 offset:0\n\t"
      "v_add_u32 v10, 0xfffe8000, v4\n\t"
      "s_waitcnt lgkmcnt(9)\n\t"
      "v_mfma_f32_16x16x32_bf16 v[128:131], v[80:83], v[52:55], v[128:131]\n\t"
      "ds_read_b128 v[100:103], v7 offset:8192\n\t"
      "v_add_u32 v11, 0xfffe8000, v5\n\t"
      "s_waitcnt lgkmcnt(9)\n\t"
      "v_mfma_f32_16x16x32_bf16 v[132:135], v[32:35], v[56:59], v[132:135]\n\t"
      "ds_read_b128 v[104:107], v7 offset:1024\n\t"
      "v_add_u32 v12, 0xfffe8000, v6\n\t"
      "s_waitcnt lgkmcnt(9)\n\t"
      "v_mfma_f32_16x16x32_bf16 v[132:135], v[80:83], v[60:63], v[132:135]\n\t"
      "ds_read_b128 v[108:111], v7 offset:9216\n\t"
      "v_add_u32 v13, 0xfffe8000, v7\n\t"
      "s_waitcnt lgkmcnt(9)\n\t"
      "v_mfma_f32_16x16x32_bf16 v[136:139], v[32:35], v[64:67], v[136:139]\n\t"
      "ds_read_b128 v[112:115], v7 offset:2048\n\t"
      "s_waitcnt lgkmcnt(9)\n\t"
      "v_mfma_f32_16x16x32_bf16 v[136:139], v[80:83], v[68:71], v[136:139]\n\t"
      "ds_read_b128 v[116:119], v7 offset:10240\n\t"
      "s_waitcnt lgkmcnt(9)\n\t"
      "v_mfma_f32_16x16x32_bf16 v[140:143], v[32:35], v[72:75], v[140:143]\n\t"
      "ds_read_b128 v[120:123], v7 offset:3072\n\t"
      "s_waitcnt lgkmcnt(9)\n\t"
      "v_mfma_f32_16x16x32_bf16 v[140:143], v[80:83], v[76:79], v[140:143]\n\t"
      "ds_read_b128 v[124:127], v7 offset:11264\n\t"
      "s_waitcnt lgkmcnt(7)\n\t"
      "v_mfma_f32_16x16x32_bf16 v[144:147], v[32:35], v[96:99], v[144:147]\n\t"
      "s_barrier\n\t"
      "ds_read_b128 v[40:43], v10 offset:0\n\t"
      "s_waitcnt lgkmcnt(7)\n\t"
      "v_mfma_f32_16x16x32_bf16 v[144:147], v[80:83], v[100:103], v[144:147]\n\t"
      "ds_read_b128 v[88:91], v11 offset:0\n\t"
      "s_waitcnt lgkmcnt(7)\n\t"
      "v_mfma_f32_16x16x32_bf16 v[148:151], v[32:35], v[104:107], v[148:151]\n\t"
      "ds_read_b128 v[48:51], v12 offset:0\n\t"
      "s_waitcnt lgkmcnt(7)\n\t"
      "v_mfma_f32_16x16x32_bf16 v[148:151], v[80:83], v[108:111], v[148:151]\n\t"
      "ds_read_b128 v[52:55], v12 offset:8192\n\t"
      "s_waitcnt lgkmcnt(7)\n\t"
      "v_mfma_f32_16x16x32_bf16 v[152:155], v[32:35], v[112:115], v[152:155]\n\t"
      "ds_read_b128 v[56:59], v12 offset:1024\n\t"
      "s_waitcnt lgkmcnt(7)\n\t"
      "v_mfma_f32_16x16x32_bf16 v[152:155], v[80:83], v[116:119], v[152:155]\n\t"
      "ds_read_b128 v[60:63], v12 offset:9216\n\t"
      "s_waitcnt lgkmcnt(7)\n\t"
      "v_mfma_f32_16x16x32_bf16 v[156:159], v[32:35], v[120:123], v[156:159]\n\t"
      "ds_read_b128 v[64:67], v12 offset:2048\n\t"
      "s_waitcnt lgkmcnt(7)\n\t"
      "v_mfma_f32_16x16x32_bf16 v[156:159], v[80:83], v[124:127], v[156:159]\n\t"
      "ds_read_b128 v[68:71], v12 offset:10240\n\t"
      "s_waitcnt lgkmcnt(5)\n\t"
      "v_mfma_f32_16x16x32_bf16 v[128:131], v[40:43], v[48:51], v[128:131]\n\t"
      "ds_read_b128 v[72:75], v12 offset:3072\n\t"
      "s_waitcnt lgkmcnt(5)\n\t"
      "v_mfma_f32_16x16x32_bf16 v[128:131], v[88:91], v[52:55], v[128:131]\n\t"
      "ds_read_b128 v[76:79], v12 offset:11264\n\t"
      "s_waitcnt lgkmcnt(5)\n\t"
      "v_mfma_f32_16x16x32_bf16 v[132:135], v[40:43], v[56:59], v[132:135]\n\t"
      "ds_read_b128 v[96:99], v13 offset:0\n\t"
      "s_waitcnt lgkmcnt(5)\n\t"
      "v_mfma_f32_16x16x32_bf16 v[132:135], v[88:91], v[60:63], v[132:135]\n\t"
      "ds_read_b128 v[100:103], v13 offset:8192\n\t"
      "s_waitcnt lgkmcnt(5)\n\t"
      "v_mfma_f32_16x16x32_bf16 v[136:139], v[40:43], v[64:67], v[136:139]\n\t"
      "ds_read_b128 v[104:107], v13 offset:1024\n\t"
      "s_waitcnt lgkmcnt(5)\n\t"
      "v_mfma_f32_16x16x32_bf16 v[136:139], v[88:91], v[68:71], v[136:139]\n\t"
      "ds_read_b128 v[108:111], v13 offset:9216\n\t"
      "s_waitcnt lgkmcnt(5)\n\t"
      "v_mfma_f32_16x16x32_bf16 v[140:143], v[40:43], v[72:75], v[140:143]\n\t"
      "ds_read_b128 v[112:115], v13 offset:2048\n\t"
      "s_waitcnt lgkmcnt(5)\n\t"
      "v_mfma_f32_16x16x32_bf16 v[140:143], v[88:91], v[76:79], v[140:143]\n\t"
      "ds_read_b128 v[116:119], v13 offset:10240\n\t"
      "s_waitcnt lgkmcnt(5)\n\t"
      "v_mfma_f32_16x16x32_bf16 v[144:147], v[40:43], v[96:99], v[144:147]\n\t"
      "ds_read_b128 v[120:123], v13 offset:3072\n\t"
      "s_waitcnt lgkmcnt(5)\n\t"
      "v_mfma_f32_16x16x32_bf16 v[144:147], v[88:91], v[100:103], v[144:147]\n\t"
      "ds_read_b128 v[124:127], v13 offset:11264\n\t"
      "s_waitcnt lgkmcnt(5)\n\t"
      "v_mfma_f32_16x16x32_bf16 v[148:151], v[40:43], v[104:107], v[148:151]\n\t"
      "ds_read_b128 v[44:47], v10 offset:2048\n\t"
      "s_waitcnt lgkmcnt(5)\n\t"
      "v_mfma_f32_16x16x32_bf16 v[148:151], v[88:91], v[108:111], v[148:151]\n\t"
      "ds_read_b128 v[92:95], v11 offset:2048\n\t"
      "s_waitcnt lgkmcnt(5)\n\t"
      "v_mfma_f32_16x16x32_bf16 v[152:155], v[40:43], v[112:115], v[152:155]\n\t"
      "s_waitcnt lgkmcnt(4)\n\t"
      "v_mfma_f32_16x16x32_bf16 v[152:155], v[88:91], v[116:119], v[152:155]\n\t"
      "s_waitcnt lgkmcnt(3)\n\t"
      "v_mfma_f32_16x16x32_bf16 v[156:159], v[40:43], v[120:123], v[156:159]\n\t"
      "s_waitcnt lgkmcnt(2)\n\t"
      "v_mfma_f32_16x16x32_bf16 v[156:159], v[88:91], v[124:127], v[156:159]\n\t"
      "s_waitcnt lgkmcnt(0)\n\t"
      "v_mfma_f32_16x16x32_bf16 v[188:191], v[92:95], v[124:127], v[188:191]\n\t"
      "v_mfma_f32_16x16x32_bf16 v[188:191], v[44:47], v[120:123], v[188:191]\n\t"
      "v_mfma_f32_16x16x32_bf16 v[184:187], v[92:95], v[116:119], v[184:187]\n\t"
      "v_mfma_f32_16x16x32_bf16 v[184:187], v[44:47], v[112:115], v[184:187]\n\t"
      "v_mfma_f32_16x16x32_bf16 v[180:183], v[92:95], v[108:111], v[180:183]\n\t"
      "v_cvt_pk_bf16_f32 v16, v128, v132\n\t"
      "v_cvt_pk_bf16_f32 v17, v136, v140\n\t"
      "v_mfma_f32_16x16x32_bf16 v[180:183], v[44:47], v[104:107], v[180:183]\n\t"
      "v_cvt_pk_bf16_f32 v18, v144, v148\n\t"
      "v_cvt_pk_bf16_f32 v19, v152, v156\n\t"
      "s_mov_b32 s48, 0\n\t"
      "buffer_store_dwordx4 v[16:19], v8, s[28:31], s48 offen nt\n\t"
      "v_mfma_f32_16x16x32_bf16 v[176:179], v[92:95], v[100:103], v[176:179]\n\t"
      "v_mfma_f32_16x16x32_bf16 v[176:179], v[44:47], v[96:99], v[176:179]\n\t"
      "v_mfma_f32_16x16x32_bf16 v[172:175], v[92:95], v[76:79], v[172:175]\n\t"
      "ds_read_b128 v[96:99], v7 offset:0\n\t"
      "v_mfma_f32_16x16x32_bf16 v[172:175], v[44:47], v[72:75], v[172:175]\n\t"
      "ds_read_b128 v[100:103], v7 offset:8192\n\t"
      "v_mfma_f32_16x16x32_bf16 v[168:171], v[92:95], v[68:71], v[168:171]\n\t"
      "ds_read_b128 v[104:107], v7 offset:1024\n\t"
      "v_cvt_pk_bf16_f32 v20, v129, v133\n\t"
      "v_cvt_pk_bf16_f32 v21, v137, v141\n\t"
      "v_mfma_f32_16x16x32_bf16 v[168:171], v[44:47], v[64:67], v[168:171]\n\t"
      "ds_read_b128 v[108:111], v7 offset:9216\n\t"
      "v_cvt_pk_bf16_f32 v22, v145, v149\n\t"
      "v_cvt_pk_bf16_f32 v23, v153, v157\n\t"
      "s_mov_b32 s48, 7168\n\t"
      "buffer_store_dwordx4 v[20:23], v8, s[28:31], s48 offen nt\n\t"
      "v_mfma_f32_16x16x32_bf16 v[164:167], v[92:95], v[60:63], v[164:167]\n\t"
      "ds_read_b128 v[112:115], v7 offset:2048\n\t"
      "v_mfma_f32_16x16x32_bf16 v[164:167], v[44:47], v[56:59], v[164:167]\n\t"
      "ds_read_b128 v[116:119], v7 offset:10240\n\t"
      "v_mfma_f32_16x16x32_bf16 v[160:163], v[92:95], v[52:55], v[160:163]\n\t"
      "ds_read_b128 v[120:123], v7 offset:3072\n\t"
      "v_mfma_f32_16x16x32_bf16 v[160:163], v[44:47], v[48:51], v[160:163]\n\t"
      "ds_read_b128 v[124:127], v7 offset:11264\n\t"
      "s_waitcnt lgkmcnt(7)\n\t"
      "v_mfma_f32_16x16x32_bf16 v[176:179], v[36:39], v[96:99], v[176:179]\n\t"
      "ds_read_b128 v[48:51], v6 offset:0\n\t"
      "v_cvt_pk_bf16_f32 v24, v130, v134\n\t"
      "v_cvt_pk_bf16_f32 v25, v138, v142\n\t"
      "s_waitcnt lgkmcnt(7)\n\t"
      "v_mfma_f32_16x16x32_bf16 v[176:179], v[84:87], v[100:103], v[176:179]\n\t"
      "ds_read_b128 v[52:55], v6 offset:8192\n\t"
      "v_cvt_pk_bf16_f32 v26, v146, v150\n\t"
      "v_cvt_pk_bf16_f32 v27, v154, v158\n\t"
      "s_mov_b32 s48, 14336\n\t"
      "buffer_store_dwordx4 v[24:27], v8, s[28:31], s48 offen nt\n\t"
      "s_waitcnt lgkmcnt(7)\n\t"
      "v_mfma_f32_16x16x32_bf16 v[180:183], v[36:39], v[104:107], v[180:183]\n\t"
      "ds_read_b128 v[56:59], v6 offset:1024\n\t"
      "s_waitcnt lgkmcnt(7)\n\t"
      "v_mfma_f32_16x16x32_bf16 v[180:183], v[84:87], v[108:111], v[180:183]\n\t"
      "ds_read_b128 v[60:63], v6 offset:9216\n\t"
      "s_waitcnt lgkmcnt(7)\n\t"
      "v_mfma_f32_16x16x32_bf16 v[184:187], v[36:39], v[112:115], v[184:187]\n\t"
      "ds_read_b128 v[64:67], v6 offset:2048\n\t"
      "s_waitcnt lgkmcnt(7)\n\t"
      "v_mfma_f32_16x16x32_bf16 v[184:187], v[84:87], v[116:119], v[184:187]\n\t"
      "ds_read_b128 v[68:71], v6 offset:10240\n\t"
      "s_waitcnt lgkmcnt(7)\n\t"
      "v_mfma_f32_16x16x32_bf16 v[188:191], v[36:39], v[120:123], v[188:191]\n\t"
      "ds_read_b128 v[72:75], v6 offset:3072\n\t"
      "v_cvt_pk_bf16_f32 v28, v131, v135\n\t"
      "v_cvt_pk_bf16_f32 v29, v139, v143\n\t"
      "s_waitcnt lgkmcnt(7)\n\t"
      "v_mfma_f32_16x16x32_bf16 v[188:191], v[84:87], v[124:127], v[188:191]\n\t"
      "ds_read_b128 v[76:79], v6 offset:11264\n\t"
      "v_cvt_pk_bf16_f32 v30, v147, v151\n\t"
      "v_cvt_pk_bf16_f32 v31, v155, v159\n\t"
      "s_mov_b32 s48, 21504\n\t"
      "buffer_store_dwordx4 v[28:31], v8, s[28:31], s48 offen nt\n\t"
      "s_waitcnt lgkmcnt(7)\n\t"
      "v_mfma_f32_16x16x32_bf16 v[160:163], v[36:39], v[48:51], v[160:163]\n\t"
      "ds_read_b128 v[32:35], v4 offset:4096\n\t"
      "s_waitcnt lgkmcnt(7)\n\t"
      "v_mfma_f32_16x16x32_bf16 v[160:163], v[84:87], v[52:55], v[160:163]\n\t"
      "ds_read_b128 v[80:83], v5 offset:4096\n\t"
      "s_waitcnt lgkmcnt(7)\n\t"
      "v_mfma_f32_16x16x32_bf16 v[164:167], v[36:39], v[56:59], v[164:167]\n\t"
      "s_waitcnt lgkmcnt(6)\n\t"
      "v_mfma_f32_16x16x32_bf16 v[164:167], v[84:87], v[60:63], v[164:167]\n\t"
      "s_waitcnt lgkmcnt(5)\n\t"
      "v_mfma_f32_16x16x32_bf16 v[168:171], v[36:39], v[64:67], v[168:171]\n\t"
      "s_waitcnt lgkmcnt(4)\n\t"
      "v_mfma_f32_16x16x32_bf16 v[168:171], v[84:87], v[68:71], v[168:171]\n\t"
      "s_waitcnt lgkmcnt(3)\n\t"
      "v_mfma_f32_16x16x32_bf16 v[172:175], v[36:39], v[72:75], v[172:175]\n\t"
      "s_waitcnt lgkmcnt(2)\n\t"
      "v_mfma_f32_16x16x32_bf16 v[172:175], v[84:87], v[76:79], v[172:175]\n\t"
      "s_waitcnt lgkmcnt(0)\n\t"
      "v_mfma_f32_16x16x32_bf16 v[204:207], v[80:83], v[76:79], v[204:207]\n\t"
      "ds_read_b128 v[40:43], v10 offset:4096\n\t"
      "v_mfma_f32_16x16x32_bf16 v[204:207], v[32:35], v[72:75], v[204:207]\n\t"
      "ds_read_b128 v[88:91], v11 offset:4096\n\t"
      "v_mfma_f32_16x16x32_bf16 v[200:203], v[80:83], v[68:71], v[200:203]\n\t"
      "v_mfma_f32_16x16x32_bf16 v[200:203], v[32:35], v[64:67], v[200:203]\n\t"
      "v_mfma_f32_16x16x32_bf16 v[196:199], v[80:83], v[60:63], v[196:199]\n\t"
      "v_cvt_pk_bf16_f32 v16, v160, v164\n\t"
      "v_cvt_pk_bf16_f32 v17, v168, v172\n\t"
      "v_mfma_f32_16x16x32_bf16 v[196:199], v[32:35], v[56:59], v[196:199]\n\t"
      "v_cvt_pk_bf16_f32 v18, v176, v180\n\t"
      "v_cvt_pk_bf16_f32 v19, v184, v188\n\t"
      "s_mov_b32 s48, 114688\n\t"
      "buffer_store_dwordx4 v[16:19], v8, s[28:31], s48 offen nt\n\t"
      "v_mfma_f32_16x16x32_bf16 v[192:195], v[80:83], v[52:55], v[192:195]\n\t"
      "v_mfma_f32_16x16x32_bf16 v[192:195], v[32:35], v[48:51], v[192:195]\n\t"
      "v_mfma_f32_16x16x32_bf16 v[220:223], v[80:83], v[124:127], v[220:223]\n\t"
      "ds_read_b128 v[48:51], v12 offset:0\n\t"
      "v_mfma_f32_16x16x32_bf16 v[220:223], v[32:35], v[120:123], v[220:223]\n\t"
      "ds_read_b128 v[52:55], v12 offset:8192\n\t"
      "v_mfma_f32_16x16x32_bf16 v[216:219], v[80:83], v[116:119], v[216:219]\n\t"
      "ds_read_b128 v[56:59], v12 offset:1024\n\t"
      "v_cvt_pk_bf16_f32 v20, v161, v165\n\t"
      "v_cvt_pk_bf16_f32 v21, v169, v173\n\t"
      "v_mfma_f32_16x16x32_bf16 v[216:219], v[32:35], v[112:115], v[216:219]\n\t"
      "ds_read_b128 v[60:63], v12 offset:9216\n\t"
      "v_cvt_pk_bf16_f32 v22, v177, v181\n\t"
      "v_cvt_pk_bf16_f32 v23, v185, v189\n\t"
      "s_mov_b32 s48, 121856\n\t"
      "buffer_store_dwordx4 v[20:23], v8, s[28:31], s48 offen nt\n\t"
      "v_mfma_f32_16x16x32_bf16 v[212:215], v[80:83], v[108:111], v[212:215]\n\t"
      "ds_read_b128 v[64:67], v12 offset:2048\n\t"
      "v_mfma_f32_16x16x32_bf16 v[212:215], v[32:35], v[104:107], v[212:215]\n\t"
      "ds_read_b128 v[68:71], v12 offset:10240\n\t"
      "v_mfma_f32_16x16x32_bf16 v[208:211], v[80:83], v[100:103], v[208:211]\n\t"
      "ds_read_b128 v[72:75], v12 offset:3072\n\t"
      "v_mfma_f32_16x16x32_bf16 v[208:211], v[32:35], v[96:99], v[208:211]\n\t"
      "ds_read_b128 v[76:79], v12 offset:11264\n\t"
      "s_waitcnt lgkmcnt(7)\n\t"
      "v_mfma_f32_16x16x32_bf16 v[192:195], v[40:43], v[48:51], v[192:195]\n\t"
      "ds_read_b128 v[96:99], v13 offset:0\n\t"
      "v_cvt_pk_bf16_f32 v24, v162, v166\n\t"
      "v_cvt_pk_bf16_f32 v25, v170, v174\n\t"
      "s_waitcnt lgkmcnt(7)\n\t"
      "v_mfma_f32_16x16x32_bf16 v[192:195], v[88:91], v[52:55], v[192:195]\n\t"
      "ds_read_b128 v[100:103], v13 offset:8192\n\t"
      "v_cvt_pk_bf16_f32 v26, v178, v182\n\t"
      "v_cvt_pk_bf16_f32 v27, v186, v190\n\t"
      "s_mov_b32 s48, 129024\n\t"
      "buffer_store_dwordx4 v[24:27], v8, s[28:31], s48 offen nt\n\t"
      "s_waitcnt lgkmcnt(7)\n\t"
      "v_mfma_f32_16x16x32_bf16 v[196:199], v[40:43], v[56:59], v[196:199]\n\t"
      "ds_read_b128 v[104:107], v13 offset:1024\n\t"
      "s_waitcnt lgkmcnt(7)\n\t"
      "v_mfma_f32_16x16x32_bf16 v[196:199], v[88:91], v[60:63], v[196:199]\n\t"
      "ds_read_b128 v[108:111], v13 offset:9216\n\t"
      "s_waitcnt lgkmcnt(7)\n\t"
      "v_mfma_f32_16x16x32_bf16 v[200:203], v[40:43], v[64:67], v[200:203]\n\t"
      "ds_read_b128 v[112:115], v13 offset:2048\n\t"
      "s_waitcnt lgkmcnt(7)\n\t"
      "v_mfma_f32_16x16x32_bf16 v[200:203], v[88:91], v[68:71], v[200:203]\n\t"
      "ds_read_b128 v[116:119], v13 offset:10240\n\t"
      "s_waitcnt lgkmcnt(7)\n\t"
      "v_mfma_f32_16x16x32_bf16 v[204:207], v[40:43], v[72:75], v[204:207]\n\t"
      "ds_read_b128 v[120:123], v13 offset:3072\n\t"
      "v_cvt_pk_bf16_f32 v28, v163, v167\n\t"
      "v_cvt_pk_bf16_f32 v29, v171, v175\n\t"
      "s_waitcnt lgkmcnt(7)\n\t"
      "v_mfma_f32_16x16x32_bf16 v[204:207], v[88:91], v[76:79], v[204:207]\n\t"
      "ds_read_b128 v[124:127], v13 offset:11264\n\t"
      "v_cvt_pk_bf16_f32 v30, v179, v183\n\t"
      "v_cvt_pk_bf16_f32 v31, v187, v191\n\t"
      "s_mov_b32 s48, 136192\n\t"
      "buffer_store_dwordx4 v[28:31], v8, s[28:31], s48 offen nt\n\t"
      "s_waitcnt lgkmcnt(7)\n\t"
      "v_mfma_f32_16x16x32_bf16 v[208:211], v[40:43], v[96:99], v[208:211]\n\t"
      "ds_read_b128 v[44:47], v10 offset:6144\n\t"
      "s_waitcnt lgkmcnt(7)\n\t"
      "v_mfma_f32_16x16x32_bf16 v[208:211], v[88:91], v[100:103], v[208:211]\n\t"
      "ds_read_b128 v[92:95], v11 offset:6144\n\t"
      "s_waitcnt lgkmcnt(7)\n\t"
      "v_mfma_f32_16x16x32_bf16 v[212:215], v[40:43], v[104:107], v[212:215]\n\t"
      "s_waitcnt lgkmcnt(6)\n\t"
      "v_mfma_f32_16x16x32_bf16 v[212:215], v[88:91], v[108:111], v[212:215]\n\t"
      "s_waitcnt lgkmcnt(5)\n\t"
      "v_mfma_f32_16x16x32_bf16 v[216:219], v[40:43], v[112:115], v[216:219]\n\t"
      "s_waitcnt lgkmcnt(4)\n\t"
      "v_mfma_f32_16x16x32_bf16 v[216:219], v[88:91], v[116:119], v[216:219]\n\t"
      "s_waitcnt lgkmcnt(3)\n\t"
      "v_mfma_f32_16x16x32_bf16 v[220:223], v[40:43], v[120:123], v[220:223]\n\t"
      "s_waitcnt lgkmcnt(2)\n\t"
      "v_mfma_f32_16x16x32_bf16 v[220:223], v[88:91], v[124:127], v[220:223]\n\t"
      "s_waitcnt lgkmcnt(0)\n\t"
      "v_mfma_f32_16x16x32_bf16 v[252:255], v[92:95], v[124:127], v[252:255]\n\t"
      "ds_read_b128 v[36:39], v4 offset:6144\n\t"
      "v_mfma_f32_16x16x32_bf16 v[252:255], v[44:47], v[120:123], v[252:255]\n\t"
      "ds_read_b128 v[84:87], v5 offset:6144\n\t"
      "v_mfma_f32_16x16x32_bf16 v[248:251], v[92:95], v[116:119], v[248:251]\n\t"
      "v_mfma_f32_16x16x32_bf16 v[248:251], v[44:47], v[112:115], v[248:251]\n\t"
      "v_mfma_f32_16x16x32_bf16 v[244:247], v[92:95], v[108:111], v[244:247]\n\t"
      "v_cvt_pk_bf16_f32 v16, v192, v196\n\t"
      "v_cvt_pk_bf16_f32 v17, v200, v204\n\t"
      "v_mfma_f32_16x16x32_bf16 v[244:247], v[44:47], v[104:107], v[244:247]\n\t"
      "v_cvt_pk_bf16_f32 v18, v208, v212\n\t"
      "v_cvt_pk_bf16_f32 v19, v216, v220\n\t"
      "s_mov_b32 s48, 229376\n\t"
      "buffer_store_dwordx4 v[16:19], v8, s[28:31], s48 offen nt\n\t"
      "v_mfma_f32_16x16x32_bf16 v[240:243], v[92:95], v[100:103], v[240:243]\n\t"
      "v_mfma_f32_16x16x32_bf16 v[240:243], v[44:47], v[96:99], v[240:243]\n\t"
      "v_mfma_f32_16x16x32_bf16 v[236:239], v[92:95], v[76:79], v[236:239]\n\t"
      "ds_read_b128 v[96:99], v7 offset:0\n\t"
      "v_mfma_f32_16x16x32_bf16 v[236:239], v[44:47], v[72:75], v[236:239]\n\t"
      "ds_read_b128 v[100:103], v7 offset:8192\n\t"
      "v_mfma_f32_16x16x32_bf16 v[232:235], v[92:95], v[68:71], v[232:235]\n\t"
      "ds_read_b128 v[104:107], v7 offset:1024\n\t"
      "v_cvt_pk_bf16_f32 v20, v193, v197\n\t"
      "v_cvt_pk_bf16_f32 v21, v201, v205\n\t"
      "v_mfma_f32_16x16x32_bf16 v[232:235], v[44:47], v[64:67], v[232:235]\n\t"
      "ds_read_b128 v[108:111], v7 offset:9216\n\t"
      "v_cvt_pk_bf16_f32 v22, v209, v213\n\t"
      "v_cvt_pk_bf16_f32 v23, v217, v221\n\t"
      "s_mov_b32 s48, 236544\n\t"
      "buffer_store_dwordx4 v[20:23], v8, s[28:31], s48 offen nt\n\t"
      "v_mfma_f32_16x16x32_bf16 v[228:231], v[92:95], v[60:63], v[228:231]\n\t"
      "ds_read_b128 v[112:115], v7 offset:2048\n\t"
      "v_mfma_f32_16x16x32_bf16 v[228:231], v[44:47], v[56:59], v[228:231]\n\t"
      "ds_read_b128 v[116:119], v7 offset:10240\n\t"
      "v_mfma_f32_16x16x32_bf16 v[224:227], v[92:95], v[52:55], v[224:227]\n\t"
      "ds_read_b128 v[120:123], v7 offset:3072\n\t"
      "v_mfma_f32_16x16x32_bf16 v[224:227], v[44:47], v[48:51], v[224:227]\n\t"
      "ds_read_b128 v[124:127], v7 offset:11264\n\t"
      "s_waitcnt lgkmcnt(7)\n\t"
      "v_mfma_f32_16x16x32_bf16 v[240:243], v[36:39], v[96:99], v[240:243]\n\t"
      "ds_read_b128 v[48:51], v6 offset:0\n\t"
      "v_cvt_pk_bf16_f32 v24, v194, v198\n\t"
      "v_cvt_pk_bf16_f32 v25, v202, v206\n\t"
      "s_waitcnt lgkmcnt(7)\n\t"
      "v_mfma_f32_16x16x32_bf16 v[240:243], v[84:87], v[100:103], v[240:243]\n\t"
      "ds_read_b128 v[52:55], v6 offset:8192\n\t"
      "v_cvt_pk_bf16_f32 v26, v210, v214\n\t"
      "v_cvt_pk_bf16_f32 v27, v218, v222\n\t"
      "s_mov_b32 s48, 243712\n\t"
      "buffer_store_dwordx4 v[24:27], v8, s[28:31], s48 offen nt\n\t"
      "s_waitcnt lgkmcnt(7)\n\t"
      "v_mfma_f32_16x16x32_bf16 v[244:247], v[36:39], v[104:107], v[244:247]\n\t"
      "ds_read_b128 v[56:59], v6 offset:1024\n\t"
      "s_waitcnt lgkmcnt(7)\n\t"
      "v_mfma_f32_16x16x32_bf16 v[244:247], v[84:87], v[108:111], v[244:247]\n\t"
      "ds_read_b128 v[60:63], v6 offset:9216\n\t"
      "s_waitcnt lgkmcnt(7)\n\t"
      "v_mfma_f32_16x16x32_bf16 v[248:251], v[36:39], v[112:115], v[248:251]\n\t"
      "ds_read_b128 v[64:67], v6 offset:2048\n\t"
      "s_waitcnt lgkmcnt(7)\n\t"
      "v_mfma_f32_16x16x32_bf16 v[248:251], v[84:87], v[116:119], v[248:251]\n\t"
      "ds_read_b128 v[68:71], v6 offset:10240\n\t"
      "s_waitcnt lgkmcnt(7)\n\t"
      "v_mfma_f32_16x16x32_bf16 v[252:255], v[36:39], v[120:123], v[252:255]\n\t"
      "ds_read_b128 v[72:75], v6 offset:3072\n\t"
      "v_cvt_pk_bf16_f32 v28, v195, v199\n\t"
      "v_cvt_pk_bf16_f32 v29, v203, v207\n\t"
      "s_waitcnt lgkmcnt(7)\n\t"
      "v_mfma_f32_16x16x32_bf16 v[252:255], v[84:87], v[124:127], v[252:255]\n\t"
      "ds_read_b128 v[76:79], v6 offset:11264\n\t"
      "v_cvt_pk_bf16_f32 v30, v211, v215\n\t"
      "v_cvt_pk_bf16_f32 v31, v219, v223\n\t"
      "s_mov_b32 s48, 250880\n\t"
      "buffer_store_dwordx4 v[28:31], v8, s[28:31], s48 offen nt\n\t"
      "s_waitcnt lgkmcnt(7)\n\t"
      "v_mfma_f32_16x16x32_bf16 v[224:227], v[36:39], v[48:51], v[224:227]\n\t"
      "s_waitcnt lgkmcnt(6)\n\t"
      "v_mfma_f32_16x16x32_bf16 v[224:227], v[84:87], v[52:55], v[224:227]\n\t"
      "s_waitcnt lgkmcnt(5)\n\t"
      "v_mfma_f32_16x16x32_bf16 v[228:231], v[36:39], v[56:59], v[228:231]\n\t"
      "s_waitcnt lgkmcnt(4)\n\t"
      "v_mfma_f32_16x16x32_bf16 v[228:231], v[84:87], v[60:63], v[228:231]\n\t"
      "s_waitcnt lgkmcnt(3)\n\t"
      "v_mfma_f32_16x16x32_bf16 v[232:235], v[36:39], v[64:67], v[232:235]\n\t"
      "s_waitcnt lgkmcnt(2)\n\t"
      "v_mfma_f32_16x16x32_bf16 v[232:235], v[84:87], v[68:71], v[232:235]\n\t"
      "s_waitcnt lgkmcnt(1)\n\t"
      "v_mfma_f32_16x16x32_bf16 v[236:239], v[36:39], v[72:75], v[236:239]\n\t"
      "s_waitcnt lgkmcnt(0)\n\t"
      "v_mfma_f32_16x16x32_bf16 v[236:239], v[84:87], v[76:79], v[236:239]\n\t"
      "s_nop 7\n\t"
      "v_cvt_pk_bf16_f32 v16, v224, v228\n\t"
      "v_cvt_pk_bf16_f32 v17, v232, v236\n\t"
      "v_cvt_pk_bf16_f32 v18, v240, v244\n\t"
      "v_cvt_pk_bf16_f32 v19, v248, v252\n\t"
      "s_mov_b32 s48, 344064\n\t"
      "buffer_store_dwordx4 v[16:19], v8, s[28:31], s48 offen nt\n\t"
      "v_cvt_pk_bf16_f32 v20, v225, v229\n\t"
      "v_cvt_pk_bf16_f32 v21, v233, v237\n\t"
      "v_cvt_pk_bf16_f32 v22, v241, v245\n\t"
      "v_cvt_pk_bf16_f32 v23, v249, v253\n\t"
      "s_mov_b32 s48, 351232\n\t"
      "buffer_store_dwordx4 v[20:23], v8, s[28:31], s48 offen nt\n\t"
      "v_cvt_pk_bf16_f32 v24, v226, v230\n\t"
      "v_cvt_pk_bf16_f32 v25, v234, v238\n\t"
      "v_cvt_pk_bf16_f32 v26, v242, v246\n\t"
      "v_cvt_pk_bf16_f32 v27, v250, v254\n\t"
      "s_mov_b32 s48, 358400\n\t"
      "buffer_store_dwordx4 v[24:27], v8, s[28:31], s48 offen nt\n\t"
      "v_cvt_pk_bf16_f32 v28, v227, v231\n\t"
      "v_cvt_pk_bf16_f32 v29, v235, v239\n\t"
      "v_cvt_pk_bf16_f32 v30, v243, v247\n\t"
      "v_cvt_pk_bf16_f32 v31, v251, v255\n\t"
      "s_mov_b32 s48, 365568\n\t"
      "buffer_store_dwordx4 v[28:31], v8, s[28:31], s48 offen nt\n\t"
      "s_endpgm\n\t"
      ".Ltend%=:\n\t"
      ".p2align 12\n\t"
      ".Lhelp%=:\n\t"
      "v_and_b32 v2, 63, %[tid]\n\t"
      "s_mov_b32 s14, 14680064\n\t"
      "s_mov_b32 s15, 0x20000\n\t"
      "s_mov_b32 s18, 114688\n\t"
      "s_mov_b32 s19, 0x20000\n\t"
      "s_mov_b32 s22, 114688\n\t"
      "s_mov_b32 s23, 0x20000\n\t"
      "s_mov_b32 s26, 2048\n\t"
      "s_mov_b32 s27, 0x20000\n\t"
      "s_mov_b32 s42, 243712\n\t"
      "s_mov_b32 s43, 0x20000\n\t"
      "s_mov_b32 s38, 7340032\n\t"
      "s_mov_b32 s39, 0x20000\n\t"
      "v_and_b32 v3, 31, v2\n\t"
      "v_lshrrev_b32 v50, 5, v2\n\t"
      "v_mul_u32_u24 v32, 114688, v50\n\t"
      "v_lshl_add_u32 v32, v3, 4, v32\n\t"
      "v_lshlrev_b32 v48, 3, v3\n\t"
      "v_lshlrev_b32 v49, 4, v50\n\t"
      "v_add_u32 v33, 14336, v32\n\t"
      "v_add_u32 v34, 28672, v32\n\t"
      "v_add_u32 v35, 43008, v32\n\t"
      "v_add_u32 v36, 57344, v32\n\t"
      "v_add_u32 v37, 71680, v32\n\t"
      "v_add_u32 v38, 86016, v32\n\t"
      "v_add_u32 v39, 100352, v32\n\t"
      "v_and_b32 v51, 7, v2\n\t"
      "v_lshlrev_b32 v51, 3, v51\n\t"
      "v_xor_b32 v50, v50, v2\n\t"
      "v_lshlrev_b32 v50, 4, v50\n\t"
      "v_cmp_gt_u32 vcc, 8, v2\n\t"
      "v_lshlrev_b32 v52, 3, v2\n\t"
      "s_nop 0\n\t"
      "v_cndmask_b32 v52, 2.0, v52, vcc\n\t"
      "v_mov_b32 v12, 0\n\t"
      "v_mov_b32 v13, 8\n\t"
      "ds_write_b32 v12, v13\n\t"
      "s_mov_b32 s10, 4194304\n\t"
      "s_mov_b32 s11, 0x20000\n\t"
      "s_and_b32 s48, %[bid], 1\n\t"
      "s_lshl_b32 s46, s48, 2\n\t"
      "s_bfe_u32 s50, %[bid], 0x20001\n\t"
      "s_add_u32 s46, s46, s50\n\t"
      "s_lshr_b32 s48, s46, 2\n\t"
      "s_bfe_u32 s49, s46, 0x10001\n\t"
      "s_mul_i32 s52, s48, 0x44444444\n\t"
      "s_mul_i32 s50, s49, 0x20020022\n\t"
      "s_xor_b32 s52, s52, s50\n\t"
      "s_xor_b32 s52, s52, 0x37526410\n\t"
      "s_mov_b32 s53, 0xfedcba98\n\t"
      "s_lshl_b32 s49, s46, 2\n\t"
      "s_lshr_b32 s50, %[bid], 3\n\t"
      "s_add_u32 s49, s49, s50\n\t"
      "s_lshl_b32 s49, s49, 17\n\t"
      "s_lshl_b32 s48, s44, 8\n\t"
      "s_add_u32 s49, s49, s48\n\t"
      "v_and_b32 v10, 7, v2\n\t"
      "v_lshl_add_u32 v10, v10, 11, s49\n\t"
      "v_lshrrev_b32 v11, 3, v2\n\t"
      "v_cmp_gt_u32 vcc, 4, v11\n\t"
      "v_lshlrev_b32 v11, 2, v11\n\t"
      "v_bfe_u32 v11, s52, v11, 4\n\t"
      "v_lshl_add_u32 v10, v11, 7, v10\n\t"
      "v_cndmask_b32 v10, 2.0, v10, vcc\n\t"
      "s_waitcnt lgkmcnt(0)\n\t"
      "s_mov_b64 s[8:9], s[72:73]\n\t"
      "s_mov_b64 s[40:41], s[86:87]\n\t"
      "s_mov_b64 s[16:17], s[76:77]\n\t"
      "s_mov_b64 s[20:21], s[78:79]\n\t"
      "s_mov_b64 s[24:25], s[80:81]\n\t"
      "s_mov_b64 s[12:13], s[74:75]\n\t"
      "s_mov_b64 s[36:37], s[84:85]\n\t"
      "buffer_load_dword v11, v10, s[8:11], 0 offen\n\t"
      "s_mul_i32 s48, %[bid], 7168\n\t"
      "s_cmp_eq_u32 s44, 0\n\t"
      "s_cselect_b32 s49, 56, 0\n\t"
      "v_cmp_gt_u32 vcc, s49, v2\n\t"
      "v_lshl_add_u32 v10, v2, 7, s48\n\t"
      "s_nop 0\n\t"
      "v_cndmask_b32 v10, 2.0, v10, vcc\n\t"
      "buffer_load_dword v17, v10, s[40:43], 0 offen sc1\n\t"
      "v_mov_b32 v13, 1\n\t"
      "s_barrier\n\t"
      "s_sleep 16\n\t"
      "s_lshr_b32 s82, s44, 6\n\t"
      "s_and_b32 s48, s82, 3\n\t"
      "s_lshr_b32 s82, s82, 2\n\t"
      "s_lshl_b32 s82, s82, 5\n\t"
      "s_add_u32 s82, s82, %[bid]\n\t"
      "s_lshl_b32 s82, s82, 2\n\t"
      "s_add_u32 s82, s82, s48\n\t"
      "s_mul_i32 s56, s82, 1171\n\t"
      "s_lshr_b32 s56, s56, 17\n\t"
      "s_mul_i32 s48, s56, 112\n\t"
      "s_sub_u32 s48, s82, s48\n\t"
      "s_add_u32 s56, s56, 8\n\t"
      "s_and_b32 s47, s48, 3\n\t"
      "s_bfe_u32 s45, s48, 0x20002\n\t"
      "s_mul_i32 s45, s45, 7\n\t"
      "s_lshr_b32 s48, s48, 4\n\t"
      "s_add_u32 s45, s45, s48\n\t"
      "s_lshl_b32 s64, s56, 2\n\t"
      "s_add_u32 s64, s64, s47\n\t"
      "s_mul_i32 s59, s64, 229376\n\t"
      "s_lshl_b32 s48, s45, 9\n\t"
      "s_add_u32 s59, s59, s48\n\t"
      "s_mul_i32 s63, s56, 7168\n\t"
      "s_lshl_b32 s48, s45, 8\n\t"
      "s_add_u32 s63, s63, s48\n\t"
      "s_lshl_b32 s64, s64, 5\n\t"
      "buffer_load_dwordx2 v[192:193], v48, s[16:19], s63 offen\n\t"
      "buffer_load_dwordx2 v[194:195], v48, s[20:23], s63 offen\n\t"
      "buffer_load_dwordx4 v[196:199], v49, s[24:27], s64 offen\n\t"
      "buffer_load_dwordx4 v[64:67], v32, s[12:15], s59 offen nt\n\t"
      "buffer_load_dwordx4 v[68:71], v33, s[12:15], s59 offen nt\n\t"
      "buffer_load_dwordx4 v[72:75], v34, s[12:15], s59 offen nt\n\t"
      "buffer_load_dwordx4 v[76:79], v35, s[12:15], s59 offen nt\n\t"
      "buffer_load_dwordx4 v[80:83], v36, s[12:15], s59 offen nt\n\t"
      "buffer_load_dwordx4 v[84:87], v37, s[12:15], s59 offen nt\n\t"
      "buffer_load_dwordx4 v[88:91], v38, s[12:15], s59 offen nt\n\t"
      "buffer_load_dwordx4 v[92:95], v39, s[12:15], s59 offen nt\n\t"
      "s_lshl_b32 s48, s45, 2\n\t"
      "s_add_u32 s48, s48, s47\n\t"
      "s_lshl_b32 s48, s48, 7\n\t"
      "s_add_u32 s74, s48, 229376\n\t"
      "buffer_load_dwordx2 v[56:57], v51, s[40:43], s74 offen sc1\n\t"
      "s_lshl_b32 s48, s45, 4\n\t"
      "s_add_u32 s48, s48, s56\n\t"
      "s_lshl_b32 s48, s48, 2\n\t"
      "s_add_u32 s48, s48, s47\n\t"
      "s_lshl_b32 s80, s48, 12\n\t"
      "s_lshl_b32 s48, s45, 2\n\t"
      "s_add_u32 s48, s48, s47\n\t"
      "s_lshl_b32 s48, s48, 4\n\t"
      "s_add_u32 s48, s48, s56\n\t"
      "s_lshl_b32 s81, s48, 7\n\t"
      "s_waitcnt vmcnt(0)\n\t"
      ".Lh_loop%=:\n\t"
      "v_add_u32 v54, 1, v56\n\t"
      "v_mov_b32 v55, v57\n\t"
      "s_mov_b32 s78, s80\n\t"
      "s_mov_b32 s79, s81\n\t"
      "v_lshlrev_b32 v224, 16, v192\n\t"
      "v_and_b32 v225, 0xffff0000, v192\n\t"
      "v_lshlrev_b32 v228, 16, v194\n\t"
      "v_and_b32 v229, 0xffff0000, v194\n\t"
      "v_lshlrev_b32 v226, 16, v193\n\t"
      "v_and_b32 v227, 0xffff0000, v193\n\t"
      "v_lshlrev_b32 v230, 16, v195\n\t"
      "v_and_b32 v231, 0xffff0000, v195\n\t"
      "v_lshlrev_b32 v232, 16, v196\n\t"
      "v_and_b32 v233, 0xffff0000, v196\n\t"
      "v_lshlrev_b32 v234, 16, v197\n\t"
      "v_and_b32 v235, 0xffff0000, v197\n\t"
      "v_lshlrev_b32 v236, 16, v198\n\t"
      "v_and_b32 v237, 0xffff0000, v198\n\t"
      "v_lshlrev_b32 v238, 16, v199\n\t"
      "v_and_b32 v239, 0xffff0000, v199\n\t"
      "v_mul_f32 v228, -v228, v224\n\t"
      "v_mul_f32 v229, -v229, v225\n\t"
      "v_mul_f32 v230, -v230, v226\n\t"
      "v_mul_f32 v231, -v231, v227\n\t"
      "v_cvt_f32_i32 v64, v64\n\t"
      "v_cvt_f32_i32 v68, v68\n\t"
      "v_cvt_f32_i32 v72, v72\n\t"
      "v_cvt_f32_i32 v76, v76\n\t"
      "v_cvt_f32_i32 v65, v65\n\t"
      "v_cvt_f32_i32 v69, v69\n\t"
      "v_cvt_f32_i32 v73, v73\n\t"
      "v_cvt_f32_i32 v77, v77\n\t"
      "v_fma_f32 v64, v64, v224, v228\n\t"
      "v_fma_f32 v68, v68, v224, v228\n\t"
      "v_fma_f32 v72, v72, v224, v228\n\t"
      "v_fma_f32 v76, v76, v224, v228\n\t"
      "v_fma_f32 v65, v65, v225, v229\n\t"
      "v_fma_f32 v69, v69, v225, v229\n\t"
      "v_fma_f32 v73, v73, v225, v229\n\t"
      "v_fma_f32 v77, v77, v225, v229\n\t"
      "v_mul_f32 v64, v64, v232\n\t"
      "v_mul_f32 v68, v68, v233\n\t"
      "v_mul_f32 v72, v72, v234\n\t"
      "v_mul_f32 v76, v76, v235\n\t"
      "v_mul_f32 v65, v65, v232\n\t"
      "v_mul_f32 v69, v69, v233\n\t"
      "v_mul_f32 v73, v73, v234\n\t"
      "v_mul_f32 v77, v77, v235\n\t"
      "v_cvt_pk_bf16_f32 v240, v64, v68\n\t"
      "v_cvt_pk_bf16_f32 v241, v72, v76\n\t"
      "v_cvt_pk_bf16_f32 v244, v65, v69\n\t"
      "v_cvt_pk_bf16_f32 v245, v73, v77\n\t"
      "v_cvt_f32_i32 v66, v66\n\t"
      "v_cvt_f32_i32 v70, v70\n\t"
      "v_cvt_f32_i32 v74, v74\n\t"
      "v_cvt_f32_i32 v78, v78\n\t"
      "v_cvt_f32_i32 v67, v67\n\t"
      "v_cvt_f32_i32 v71, v71\n\t"
      "v_cvt_f32_i32 v75, v75\n\t"
      "v_cvt_f32_i32 v79, v79\n\t"
      "v_fma_f32 v66, v66, v226, v230\n\t"
      "v_fma_f32 v70, v70, v226, v230\n\t"
      "v_fma_f32 v74, v74, v226, v230\n\t"
      "v_fma_f32 v78, v78, v226, v230\n\t"
      "v_fma_f32 v67, v67, v227, v231\n\t"
      "v_fma_f32 v71, v71, v227, v231\n\t"
      "v_fma_f32 v75, v75, v227, v231\n\t"
      "v_fma_f32 v79, v79, v227, v231\n\t"
      "v_mul_f32 v66, v66, v232\n\t"
      "v_mul_f32 v70, v70, v233\n\t"
      "v_mul_f32 v74, v74, v234\n\t"
      "v_mul_f32 v78, v78, v235\n\t"
      "v_mul_f32 v67, v67, v232\n\t"
      "v_mul_f32 v71, v71, v233\n\t"
      "v_mul_f32 v75, v75, v234\n\t"
      "v_mul_f32 v79, v79, v235\n\t"
      "v_cvt_pk_bf16_f32 v248, v66, v70\n\t"
      "v_cvt_pk_bf16_f32 v249, v74, v78\n\t"
      "v_cvt_pk_bf16_f32 v252, v67, v71\n\t"
      "v_cvt_pk_bf16_f32 v253, v75, v79\n\t"
      "v_cvt_f32_i32 v80, v80\n\t"
      "v_cvt_f32_i32 v84, v84\n\t"
      "v_cvt_f32_i32 v88, v88\n\t"
      "v_cvt_f32_i32 v92, v92\n\t"
      "v_cvt_f32_i32 v81, v81\n\t"
      "v_cvt_f32_i32 v85, v85\n\t"
      "v_cvt_f32_i32 v89, v89\n\t"
      "v_cvt_f32_i32 v93, v93\n\t"
      "v_fma_f32 v80, v80, v224, v228\n\t"
      "v_fma_f32 v84, v84, v224, v228\n\t"
      "v_fma_f32 v88, v88, v224, v228\n\t"
      "v_fma_f32 v92, v92, v224, v228\n\t"
      "v_fma_f32 v81, v81, v225, v229\n\t"
      "v_fma_f32 v85, v85, v225, v229\n\t"
      "v_fma_f32 v89, v89, v225, v229\n\t"
      "v_fma_f32 v93, v93, v225, v229\n\t"
      "v_mul_f32 v80, v80, v236\n\t"
      "v_mul_f32 v84, v84, v237\n\t"
      "v_mul_f32 v88, v88, v238\n\t"
      "v_mul_f32 v92, v92, v239\n\t"
      "v_mul_f32 v81, v81, v236\n\t"
      "v_mul_f32 v85, v85, v237\n\t"
      "v_mul_f32 v89, v89, v238\n\t"
      "v_mul_f32 v93, v93, v239\n\t"
      "v_cvt_pk_bf16_f32 v242, v80, v84\n\t"
      "v_cvt_pk_bf16_f32 v243, v88, v92\n\t"
      "buffer_store_dwordx4 v[240:243], v50, s[36:39], s78 offen offset:0 sc1\n\t"
      "v_cvt_pk_bf16_f32 v246, v81, v85\n\t"
      "v_cvt_pk_bf16_f32 v247, v89, v93\n\t"
      "buffer_store_dwordx4 v[244:247], v50, s[36:39], s78 offen offset:1024 sc1\n\t"
      "v_cvt_f32_i32 v82, v82\n\t"
      "v_cvt_f32_i32 v86, v86\n\t"
      "v_cvt_f32_i32 v90, v90\n\t"
      "v_cvt_f32_i32 v94, v94\n\t"
      "s_mov_b64 exec, 1\n\t"
      "ds_add_rtn_u32 v14, v12, v13\n\t"
      "s_mov_b64 exec, -1\n\t"
      "v_cvt_f32_i32 v83, v83\n\t"
      "v_cvt_f32_i32 v87, v87\n\t"
      "v_cvt_f32_i32 v91, v91\n\t"
      "v_cvt_f32_i32 v95, v95\n\t"
      "v_fma_f32 v82, v82, v226, v230\n\t"
      "v_fma_f32 v86, v86, v226, v230\n\t"
      "v_fma_f32 v90, v90, v226, v230\n\t"
      "v_fma_f32 v94, v94, v226, v230\n\t"
      "v_fma_f32 v83, v83, v227, v231\n\t"
      "v_fma_f32 v87, v87, v227, v231\n\t"
      "v_fma_f32 v91, v91, v227, v231\n\t"
      "v_fma_f32 v95, v95, v227, v231\n\t"
      "v_mul_f32 v82, v82, v236\n\t"
      "v_mul_f32 v86, v86, v237\n\t"
      "v_mul_f32 v90, v90, v238\n\t"
      "v_mul_f32 v94, v94, v239\n\t"
      "v_mul_f32 v83, v83, v236\n\t"
      "v_mul_f32 v87, v87, v237\n\t"
      "v_mul_f32 v91, v91, v238\n\t"
      "v_mul_f32 v95, v95, v239\n\t"
      "v_cvt_pk_bf16_f32 v250, v82, v86\n\t"
      "v_cvt_pk_bf16_f32 v251, v90, v94\n\t"
      "buffer_store_dwordx4 v[248:251], v50, s[36:39], s78 offen offset:2048 sc1\n\t"
      "v_cvt_pk_bf16_f32 v254, v83, v87\n\t"
      "v_cvt_pk_bf16_f32 v255, v91, v95\n\t"
      "buffer_store_dwordx4 v[252:255], v50, s[36:39], s78 offen offset:3072 sc1\n\t"
      "s_waitcnt lgkmcnt(0)\n\t"
      "v_readfirstlane_b32 s82, v14\n\t"
      "s_nop 3\n\t"
      "s_and_b32 s48, s82, 3\n\t"
      "s_lshr_b32 s82, s82, 2\n\t"
      "s_lshl_b32 s82, s82, 5\n\t"
      "s_add_u32 s82, s82, %[bid]\n\t"
      "s_lshl_b32 s82, s82, 2\n\t"
      "s_add_u32 s82, s82, s48\n\t"
      "s_cmp_ge_u32 s82, 896\n\t"
      "s_cbranch_scc1 .Lh_last%=\n\t"
      "s_mul_i32 s56, s82, 1171\n\t"
      "s_lshr_b32 s56, s56, 17\n\t"
      "s_mul_i32 s48, s56, 112\n\t"
      "s_sub_u32 s48, s82, s48\n\t"
      "s_add_u32 s56, s56, 8\n\t"
      "s_and_b32 s47, s48, 3\n\t"
      "s_bfe_u32 s45, s48, 0x20002\n\t"
      "s_mul_i32 s45, s45, 7\n\t"
      "s_lshr_b32 s48, s48, 4\n\t"
      "s_add_u32 s45, s45, s48\n\t"
      "s_lshl_b32 s64, s56, 2\n\t"
      "s_add_u32 s64, s64, s47\n\t"
      "s_mul_i32 s59, s64, 229376\n\t"
      "s_lshl_b32 s48, s45, 9\n\t"
      "s_add_u32 s59, s59, s48\n\t"
      "s_mul_i32 s63, s56, 7168\n\t"
      "s_lshl_b32 s48, s45, 8\n\t"
      "s_add_u32 s63, s63, s48\n\t"
      "s_lshl_b32 s64, s64, 5\n\t"
      "buffer_load_dwordx2 v[192:193], v48, s[16:19], s63 offen\n\t"
      "buffer_load_dwordx2 v[194:195], v48, s[20:23], s63 offen\n\t"
      "buffer_load_dwordx4 v[196:199], v49, s[24:27], s64 offen\n\t"
      "buffer_load_dwordx4 v[64:67], v32, s[12:15], s59 offen nt\n\t"
      "buffer_load_dwordx4 v[68:71], v33, s[12:15], s59 offen nt\n\t"
      "buffer_load_dwordx4 v[72:75], v34, s[12:15], s59 offen nt\n\t"
      "buffer_load_dwordx4 v[76:79], v35, s[12:15], s59 offen nt\n\t"
      "buffer_load_dwordx4 v[80:83], v36, s[12:15], s59 offen nt\n\t"
      "buffer_load_dwordx4 v[84:87], v37, s[12:15], s59 offen nt\n\t"
      "buffer_load_dwordx4 v[88:91], v38, s[12:15], s59 offen nt\n\t"
      "buffer_load_dwordx4 v[92:95], v39, s[12:15], s59 offen nt\n\t"
      "s_lshl_b32 s48, s45, 2\n\t"
      "s_add_u32 s48, s48, s47\n\t"
      "s_lshl_b32 s48, s48, 7\n\t"
      "s_add_u32 s74, s48, 229376\n\t"
      "buffer_load_dwordx2 v[56:57], v51, s[40:43], s74 offen sc1\n\t"
      "s_lshl_b32 s48, s45, 4\n\t"
      "s_add_u32 s48, s48, s56\n\t"
      "s_lshl_b32 s48, s48, 2\n\t"
      "s_add_u32 s48, s48, s47\n\t"
      "s_lshl_b32 s80, s48, 12\n\t"
      "s_lshl_b32 s48, s45, 2\n\t"
      "s_add_u32 s48, s48, s47\n\t"
      "s_lshl_b32 s48, s48, 4\n\t"
      "s_add_u32 s48, s48, s56\n\t"
      "s_lshl_b32 s81, s48, 7\n\t"
      "s_waitcnt vmcnt(12)\n\t"
      "buffer_store_dwordx2 v[54:55], v52, s[40:43], s79 offen sc1\n\t"
      "s_waitcnt vmcnt(1)\n\t"
      "s_branch .Lh_loop%=\n\t"
      ".Lh_last%=:\n\t"
      "s_waitcnt vmcnt(0)\n\t"
      "buffer_store_dwordx2 v[54:55], v52, s[40:43], s79 offen sc1\n\t"
      "s_getpc_b64 s[88:89]\n\t"
      ".Lpc1_%=:\n\t"
      "s_add_u32 s88, s88, .Lc_tail%=-.Lpc1_%=\n\t"
      "s_addc_u32 s89, s89, -1\n\t"
      "s_mov_b32 s90, .Ltend%=-.Lc_tail%=+128\n\t"
      "s_mov_b32 s91, 0x20000\n\t"
      "v_lshlrev_b32 v16, 7, v2\n\t"
      "buffer_load_dword v17, v16, s[88:91], 0 offen\n\t"
      "s_waitcnt vmcnt(0)\n\t"
      ".Lend%=:\n\t"
      :
      : [ka] "s"(__builtin_amdgcn_kernarg_segment_ptr()), [bid] "s"(blockIdx.x), [tid] "v"(threadIdx.x),
        "i"((__attribute__((address_space(3))) char*)smem)
      :
        "v2", "v3", "v4", "v5", "v6", "v7", "v8", "v9", "v10", "v11", "v12", "v13", "v14", "v15", "v16", "v17",
        "v18", "v19", "v20", "v21", "v22", "v23", "v24", "v25", "v26", "v27", "v28", "v29", "v30", "v31", "v32", "v33",
        "v34", "v35", "v36", "v37", "v38", "v39", "v40", "v41", "v42", "v43", "v44", "v45", "v46", "v47", "v48", "v49",
        "v50", "v51", "v52", "v53", "v54", "v55", "v56", "v57", "v58", "v59", "v60", "v61", "v62", "v63", "v64", "v65",
        "v66", "v67", "v68", "v69", "v70", "v71", "v72", "v73", "v74", "v75", "v76", "v77", "v78", "v79", "v80", "v81",
        "v82", "v83", "v84", "v85", "v86", "v87", "v88", "v89", "v90", "v91", "v92", "v93", "v94", "v95", "v96", "v97",
        "v98", "v99", "v100", "v101", "v102", "v103", "v104", "v105", "v106", "v107", "v108", "v109", "v110", "v111", "v112", "v113",
        "v114", "v115", "v116", "v117", "v118", "v119", "v120", "v121", "v122", "v123", "v124", "v125", "v126", "v127", "v128", "v129",
        "v130", "v131", "v132", "v133", "v134", "v135", "v136", "v137", "v138", "v139", "v140", "v141", "v142", "v143", "v144", "v145",
        "v146", "v147", "v148", "v149", "v150", "v151", "v152", "v153", "v154", "v155", "v156", "v157", "v158", "v159", "v160", "v161",
        "v162", "v163", "v164", "v165", "v166", "v167", "v168", "v169", "v170", "v171", "v172", "v173", "v174", "v175", "v176", "v177",
        "v178", "v179", "v180", "v181", "v182", "v183", "v184", "v185", "v186", "v187", "v188", "v189", "v190", "v191", "v192", "v193",
        "v194", "v195", "v196", "v197", "v198", "v199", "v200", "v201", "v202", "v203", "v204", "v205", "v206", "v207", "v208", "v209",
        "v210", "v211", "v212", "v213", "v214", "v215", "v216", "v217", "v218", "v219", "v220", "v221", "v222", "v223", "v224", "v225",
        "v226", "v227", "v228", "v229", "v230", "v231", "v232", "v233", "v234", "v235", "v236", "v237", "v238", "v239", "v240", "v241",
        "v242", "v243", "v244", "v245", "v246", "v247", "v248", "v249", "v250", "v251", "v252", "v253", "v254", "v255", "s8", "s9",
        "s10", "s11", "s12", "s13", "s14", "s15", "s16", "s17", "s18", "s19", "s20", "s21", "s22", "s23", "s24", "s25",
        "s26", "s27", "s28", "s29", "s30", "s31", "s36", "s37", "s38", "s39", "s40", "s41", "s42", "s43", "s44", "s45",
        "s46", "s47", "s48", "s49", "s50", "s51", "s52", "s53", "s54", "s55", "s56", "s57", "s58", "s59", "s60", "s61",
        "s62", "s63", "s64", "s65", "s66", "s67", "s68", "s69", "s70", "s71", "s72", "s73", "s74", "s75", "s76", "s77",
        "s78", "s79", "s80", "s81", "s82", "s83", "s84", "s85", "s86", "s87", "s88", "s89", "s90", "s91", "vcc", "scc",
        "memory");
}

extern "C" void kernel_launch(void* const* d_in, const int* in_sizes, int n_in, void* d_out, int out_size,
                              void* d_ws, size_t ws_size, hipStream_t stream) {
  char* image = (char*)d_ws;
  wq_gemm<<<256, 512, 0, stream>>>(d_in[0], d_in[1], d_in[2], d_in[3], d_in[4], d_out, image, image + kImageBytes);
}
```
